```python
import torch

def _softmax(x, axis=-1):
    return torch.softmax(x, dim=axis)
def _gelu(x):
    return torch.nn.functional.gelu(x, approximate="tanh")
def _silu(x):
    return torch.nn.functional.silu(x)
def _sigmoid(x):
    return torch.sigmoid(x)
def _layernorm(x, axis=-1, eps=1e-5):
    m = x.mean(dim=axis, keepdim=True)
    v = x.var(dim=axis, keepdim=True, unbiased=False)
    return (x - m) / torch.sqrt(v + eps)
def _scatter_add(tgt, idx, src, axis=0):
    return tgt.clone().index_add_(axis, idx, src)
def _segment_sum(src, seg, n):
    out = torch.zeros((n,) + src.shape[1:], dtype=src.dtype)
    return out.index_add_(0, seg, src)
def _pad(x, pw):
    flat = tuple(v for pair in reversed(pw) for v in pair)
    return torch.nn.functional.pad(x, flat)

kernel_name = 'recurrent_scan__ssm_conv_s5__glu_gated_swiglu__prompt'

def setup_inputs():

    _w = (lambda a: (lambda t: t.to(torch.bfloat16) if t.is_floating_point() else t)(torch.as_tensor(a)))
    def _A_setup():
        B, S, D = 1, 2048, 640
        P = D
        Lambda_re = -0.5 * (lambda _x: torch.exp(torch.as_tensor(_x)))((lambda *s: torch.randn(*s, dtype=torch.bfloat16) * 0.8)(P))
        Lambda_im = (lambda *s: torch.randn(*s, dtype=torch.bfloat16) * 0.8)(P) * 1.0
        B_re = (lambda *s: torch.randn(*s, dtype=torch.bfloat16) * 0.8)(P, D) * (D ** -0.5)
        B_im = (lambda *s: torch.randn(*s, dtype=torch.bfloat16) * 0.8)(P, D) * (D ** -0.5)
        C_re = (lambda *s: torch.randn(*s, dtype=torch.bfloat16) * 0.8)(D, P) * (P ** -0.5)
        C_im = (lambda *s: torch.randn(*s, dtype=torch.bfloat16) * 0.8)(D, P) * (P ** -0.5)
        log_dt = (lambda *s: torch.randn(*s, dtype=torch.bfloat16) * 0.8)(P) * 0.3 - 2.0
        Dskip = (lambda *s: torch.randn(*s, dtype=torch.bfloat16) * 0.8)(D)
        return {
            "x": _w((lambda *s: torch.randn(*s, dtype=torch.bfloat16) * 0.05)(B, S, D)),
            "Lambda_re": _w(Lambda_re),
            "Lambda_im": _w(Lambda_im),
            "B_re": _w(B_re),
            "B_im": _w(B_im),
            "C_re": _w(C_re),
            "C_im": _w(C_im),
            "log_dt": _w(log_dt),
            "Dskip": _w(Dskip),
        }
    _A_in = _A_setup()

    def _B_setup():
        B, S, D, F = 1, 2048, 640, 1728
        return {
            "x": _w((lambda *s: torch.randn(*s, dtype=torch.bfloat16) * 0.05)(B, S, D)),
            "w1": _w((lambda *s: torch.randn(*s, dtype=torch.bfloat16) * 0.2)(D, F) * (2.0 / D) ** 0.5),
            "w3": _w((lambda *s: torch.randn(*s, dtype=torch.bfloat16) * 0.2)(D, F) * (2.0 / D) ** 0.5),
            "w2": _w((lambda *s: torch.randn(*s, dtype=torch.bfloat16) * 0.2)(F, D) * (2.0 / F) ** 0.5),
        }
    _B_in = _B_setup()

    return {
        "A_x": _A_in["x"],
        "A_Lambda_re": _A_in["Lambda_re"],
        "A_Lambda_im": _A_in["Lambda_im"],
        "A_B_re": _A_in["B_re"],
        "A_B_im": _A_in["B_im"],
        "A_C_re": _A_in["C_re"],
        "A_C_im": _A_in["C_im"],
        "A_log_dt": _A_in["log_dt"],
        "A_Dskip": _A_in["Dskip"],
        "B_x": _B_in["x"],
        "B_w1": _B_in["w1"],
        "B_w3": _B_in["w3"],
        "B_w2": _B_in["w2"],
    }

def reference(A_x, A_Lambda_re, A_Lambda_im, A_B_re, A_B_im, A_C_re, A_C_im, A_log_dt, A_Dskip, B_x, B_w1, B_w3, B_w2):
    def _A_ref(x, Lambda_re, Lambda_im, B_re, B_im, C_re, C_im, log_dt, Dskip):
        Bsz, L, H = x.shape
        Lam = Lambda_re + 1j * Lambda_im
        Bmat = B_re + 1j * B_im
        Cmat = C_re + 1j * C_im
        dt = torch.exp(log_dt)
        Lam_bar = torch.exp(dt * Lam)
        B_bar = ((Lam_bar - 1.0) / Lam)[:, None] * Bmat
        Bu = torch.einsum("bld,pd->blp", x + 0j, B_bar)
        h = torch.zeros_like(Bu[:, 0, :])
        hs = []
        for t in range(L):
            h = Lam_bar[None, :] * h + Bu[:, t, :]
            hs.append(h)
        hseq = torch.stack(hs, 1)
        y = 2.0 * torch.einsum("blp,dp->bld", hseq, Cmat).real + x * Dskip[None, None, :]
        return y

    def _B_ref(x, w1, w3, w2):
        gate = torch.einsum("bsd,df->bsf", x, w1)
        up = torch.einsum("bsd,df->bsf", x, w3)
        h = _silu(gate) * up
        out = torch.einsum("bsf,fd->bsd", h, w2)
        return out

    x = A_x
    xn = _layernorm(x, axis=-1)
    s = _A_ref(xn, A_Lambda_re, A_Lambda_im, A_B_re, A_B_im, A_C_re, A_C_im, A_log_dt, A_Dskip)
    g = _silu(_B_ref(xn, B_w1, B_w3, B_w2))
    return x + s * g
```

```cpp
#include <hip/hip_runtime.h>
#include <hip/hip_bf16.h>
typedef unsigned short u16;
static constexpr int L = 2048, D = 640, P = 640, F = 1728;
__device__ __forceinline__ float bf2f(u16 b) { return __uint_as_float(((unsigned)b) << 16); }
__device__ __forceinline__ u16 f2bf(float f) { unsigned u = __float_as_uint(f); u += 0x7FFFu + ((u >> 16) & 1u); return (u16)(u >> 16); }
__device__ __forceinline__ float rbf(float f) { return bf2f(f2bf(f)); }

__global__ void k_ln(const u16* x, u16* xn) {
  int row = blockIdx.x; int t = threadIdx.x;
  float v[10]; float s = 0;
  for (int i = 0; i < 10; ++i) { v[i] = bf2f(x[row * D + t + 64 * i]); s += v[i]; }
  for (int o = 32; o >= 1; o >>= 1) s += __shfl_xor(s, o);
  float mean = s / D; float q = 0;
  for (int i = 0; i < 10; ++i) { float d = v[i] - mean; q += d * d; }
  for (int o = 32; o >= 1; o >>= 1) q += __shfl_xor(q, o);
  float var = rbf(q / D); float m = rbf(mean);
  float sd = rbf(sqrtf(rbf(var + 1e-5f)));
  for (int i = 0; i < 10; ++i) xn[row * D + t + 64 * i] = f2bf(rbf(v[i] - m) / sd);
}
__global__ void k_bu(const u16* xn, const u16* Bre, const u16* Bim, const u16* Lre, const u16* Lim, const u16* ldt, float* u) {
  int p = blockIdx.x * 64 + threadIdx.x; int l = blockIdx.y;
  float ar = 0, ai = 0;
  for (int d = 0; d < D; ++d) { float xv = bf2f(xn[l * D + d]); ar += xv * bf2f(Bre[p * D + d]); ai += xv * bf2f(Bim[p * D + d]); }
  float dt = rbf(expf(bf2f(ldt[p]))); float lr = bf2f(Lre[p]), li = bf2f(Lim[p]);
  float e = expf(dt * lr); float br = e * cosf(dt * li), bi = e * sinf(dt * li);
  float nr = br - 1.0f, ni = bi; float den = lr * lr + li * li;
  float cr = (nr * lr + ni * li) / den, ci = (ni * lr - nr * li) / den;
  u[(l * P + p) * 2] = cr * ar - ci * ai; u[(l * P + p) * 2 + 1] = cr * ai + ci * ar;
}
__global__ void k_scan(float* u, const u16* Lre, const u16* Lim, const u16* ldt) {
  int p = blockIdx.x * 64 + threadIdx.x;
  float dt = rbf(expf(bf2f(ldt[p]))); float lr = bf2f(Lre[p]), li = bf2f(Lim[p]);
  float e = expf(dt * lr); float br = e * cosf(dt * li), bi = e * sinf(dt * li);
  float hr = 0, hi = 0;
  for (int l = 0; l < L; ++l) { float ur = u[(l * P + p) * 2], ui = u[(l * P + p) * 2 + 1];
    float nr = br * hr - bi * hi + ur, ni = br * hi + bi * hr + ui; hr = nr; hi = ni; u[(l * P + p) * 2] = hr; u[(l * P + p) * 2 + 1] = hi; }
}
__global__ void k_ffn1(const u16* xn, const u16* w1, const u16* w3, u16* hm) {
  int f = blockIdx.x * 64 + threadIdx.x; int l = blockIdx.y;
  float g = 0, up = 0;
  for (int d = 0; d < D; ++d) { float xv = bf2f(xn[l * D + d]); g += xv * bf2f(w1[d * F + f]); up += xv * bf2f(w3[d * F + f]); }
  g = rbf(g); up = rbf(up);
  float s = rbf(g / (1.0f + expf(-g)));
  hm[l * F + f] = f2bf(s * up);
}
__global__ void k_final(const u16* x, const u16* xn, const float* h, const u16* Cre, const u16* Cim, const u16* Dsk, const u16* hm, const u16* w2, float* out) {
  int d = blockIdx.x * 64 + threadIdx.x; int l = blockIdx.y;
  float y = 0;
  for (int p = 0; p < P; ++p) y += h[(l * P + p) * 2] * bf2f(Cre[d * P + p]) - h[(l * P + p) * 2 + 1] * bf2f(Cim[d * P + p]);
  float o = 0;
  for (int f = 0; f < F; ++f) o += bf2f(hm[l * F + f]) * bf2f(w2[f * D + d]);
  o = rbf(o); float g = rbf(o / (1.0f + expf(-o)));
  float s = 2.0f * y + rbf(bf2f(xn[l * D + d]) * bf2f(Dsk[d]));
  out[l * D + d] = bf2f(x[l * D + d]) + s * g;
}
extern "C" void kernel_launch(void* const* d_in, const int* in_sizes, int n_in, void* d_out, int out_size, void* d_ws, size_t ws_size, hipStream_t stream) {
  const u16* x = (const u16*)d_in[0]; const u16* Lre = (const u16*)d_in[1]; const u16* Lim = (const u16*)d_in[2];
  const u16* Bre = (const u16*)d_in[3]; const u16* Bim = (const u16*)d_in[4]; const u16* Cre = (const u16*)d_in[5]; const u16* Cim = (const u16*)d_in[6];
  const u16* ldt = (const u16*)d_in[7]; const u16* Dsk = (const u16*)d_in[8]; const u16* w1 = (const u16*)d_in[10]; const u16* w3 = (const u16*)d_in[11]; const u16* w2 = (const u16*)d_in[12];
  char* ws = (char*)d_ws; u16* xn = (u16*)ws; float* u = (float*)(ws + (size_t)L * D * 2); u16* hm = (u16*)(ws + (size_t)L * D * 2 + (size_t)L * P * 8);
  k_ln<<<L, 64, 0, stream>>>(x, xn);
  k_bu<<<dim3(P / 64, L), 64, 0, stream>>>(xn, Bre, Bim, Lre, Lim, ldt, u);
  k_scan<<<P / 64, 64, 0, stream>>>(u, Lre, Lim, ldt);
  k_ffn1<<<dim3(F / 64, L), 64, 0, stream>>>(xn, w1, w3, hm);
  k_final<<<dim3(D / 64, L), 64, 0, stream>>>(x, xn, u, Cre, Cim, Dsk, hm, w2, (float*)d_out);
}
```

```cpp
#include <hip/hip_runtime.h>
#include <hip/hip_bf16.h>

typedef unsigned short u16;
typedef short bf16x8 __attribute__((ext_vector_type(8)));
typedef short s16x4 __attribute__((ext_vector_type(4)));
typedef float f32x4 __attribute__((ext_vector_type(4)));
typedef float f32x2 __attribute__((ext_vector_type(2)));
typedef unsigned u32x4 __attribute__((ext_vector_type(4)));
typedef unsigned u32x2 __attribute__((ext_vector_type(2)));
typedef __bf16 bf16x2_t __attribute__((ext_vector_type(2)));
#define LAS __attribute__((address_space(3)))
#define GAS __attribute__((address_space(1)))
typedef GAS unsigned gu32;
typedef GAS unsigned long long gu64;

static constexpr int L = 2048, D = 640, P = 640, F = 1728;
static constexpr size_t WS_XN = 0;
static constexpr size_t WS_HM = WS_XN + (size_t)L * D * 2;
static constexpr size_t WS_HT = WS_HM + (size_t)16 * 27 * 16384;
static constexpr size_t WS_W2 = WS_HT + (size_t)16 * 20 * 16384;
static constexpr size_t WS_YX = WS_W2 + (size_t)8 * 27 * 10240;
static constexpr size_t WS_AG = WS_YX + (size_t)128 * 40960;
static constexpr size_t WS_FL = WS_AG + (size_t)10 * 8 * 128 * 8;
static constexpr size_t WS_END = WS_FL + (size_t)128 * 256;

struct Args {
  const u16* x; const u16* lre; const u16* lim; const u16* bre; const u16* bim; const u16* cre; const u16* cim;
  const u16* ldt; const u16* dsk; const u16* w1; const u16* w3; const u16* w2; float* out; char* ws;
};

__device__ __forceinline__ float bf2f(u16 b) { return __uint_as_float(((unsigned)b) << 16); }
__device__ __forceinline__ unsigned pk_bf16(float lo, float hi) { f32x2 v = {lo, hi}; bf16x2_t r = __builtin_convertvector(v, bf16x2_t); return __builtin_bit_cast(unsigned, r); }
__device__ __forceinline__ float bflo(unsigned p) { return __uint_as_float(p << 16); }
__device__ __forceinline__ float bfhi(unsigned p) { return __uint_as_float(p & 0xffff0000u); }
__device__ __forceinline__ float rbf(float f) { return bflo(pk_bf16(f, 0.f)); }
__device__ __forceinline__ float silu_f(float v) { return v * __builtin_amdgcn_rcpf(1.0f + __builtin_amdgcn_exp2f(v * -1.44269504089f)); }

#define BAR() asm volatile("s_barrier" ::: "memory")
#define SCHED() __builtin_amdgcn_sched_barrier(0)
#define BAR_L() do { SCHED(); asm volatile("s_waitcnt lgkmcnt(0)\n\ts_barrier" ::: "memory"); SCHED(); } while (0)
#define BAR_S() do { SCHED(); asm volatile("s_barrier" ::: "memory"); SCHED(); } while (0)
template <int ND, int NM> __device__ __forceinline__ void ilv() {
#pragma unroll
  for (int i = 0; i < ND; ++i) { __builtin_amdgcn_sched_group_barrier(0x8, 1, 0); __builtin_amdgcn_sched_group_barrier(0x100, 1, 0); }
  __builtin_amdgcn_sched_group_barrier(0x8, NM, 0); }
#define ILV(a, b) ilv<(a), (b)>()
#define WAIT_V(n) asm volatile("s_waitcnt vmcnt(" #n ")" ::: "memory")
#define MFMA16(a, b, c) __builtin_amdgcn_mfma_f32_16x16x32_bf16(a, b, c, 0, 0, 0)
__device__ __forceinline__ void glds16(const void* g, LAS char* l) { __builtin_amdgcn_global_load_lds((const unsigned*)g, (LAS unsigned*)l, 16, 0, 0); }
__device__ __forceinline__ bf16x8 ld_row(const LAS char* p) { return *(const LAS bf16x8*)p; }
typedef short v4i16_t __attribute__((ext_vector_type(4)));
__device__ __forceinline__ s16x4 ld_tr4(const LAS char* p) { return __builtin_amdgcn_ds_read_tr16_b64_v4i16((LAS v4i16_t*)p); }
__device__ __forceinline__ bf16x8 ld_tr(const LAS char* p, int t1off) { const s16x4 lo = ld_tr4(p), hi = ld_tr4(p + t1off); return __builtin_shufflevector(lo, hi, 0, 1, 2, 3, 4, 5, 6, 7); }

#define ST_DECL()
#define ST()
#define ST_RT(slot)
#define ST_OUT(kern, wid_)
__global__ __launch_bounds__(256) void k0_prep(Args a) {
  const int tid = threadIdx.x, lane = tid & 63, wid = tid >> 6, b = blockIdx.x;
  if (wid < 2) {
    const int row = b * 8 + wid * 4 + (lane >> 4), c = lane & 15;
    const u32x4* src = (const u32x4*)(a.x + (size_t)row * D);
    u32x4 v[5]; float f[40];
#pragma unroll
    for (int i = 0; i < 5; ++i) v[i] = src[c + 16 * i];
    float s = 0.f;
#pragma unroll
    for (int i = 0; i < 5; ++i)
#pragma unroll
      for (int j = 0; j < 4; ++j) { f[i * 8 + j * 2] = bflo(v[i][j]); f[i * 8 + j * 2 + 1] = bfhi(v[i][j]); s += f[i * 8 + j * 2] + f[i * 8 + j * 2 + 1]; }
#pragma unroll
    for (int o = 8; o >= 1; o >>= 1) s += __shfl_xor(s, o);
    const float mean = s * (1.0f / D); float q = 0.f;
#pragma unroll
    for (int i = 0; i < 40; ++i) { const float d = f[i] - mean; q += d * d; }
#pragma unroll
    for (int o = 8; o >= 1; o >>= 1) q += __shfl_xor(q, o);
    const float m = rbf(mean), sd = rbf(__builtin_sqrtf(rbf(rbf(q * (1.0f / D)) + 1e-5f)));
    u32x4* dst = (u32x4*)(a.ws + WS_XN + (size_t)row * D * 2);
#pragma unroll
    for (int i = 0; i < 5; ++i) { u32x4 o;
#pragma unroll
      for (int j = 0; j < 4; ++j) { const unsigned d = pk_bf16(f[i * 8 + j * 2] - m, f[i * 8 + j * 2 + 1] - m); o[j] = pk_bf16(bflo(d) / sd, bfhi(d) / sd); }
      dst[c + 16 * i] = o; }
  } else {
    const int t2 = b * 128 + (tid - 128);
    u32x4* dst = (u32x4*)(a.ws + WS_W2);
    for (int c = t2; c < 8 * 27 * 640; c += 32768) {
      const int dg = c / 17280, rem = c % 17280, s = rem / 640, cc = rem % 640;
      const int e = cc & 1, qq = (cc >> 1) & 3, g1 = (cc >> 3) & 1, r = cc >> 4, n = r % 5, ut = r / 5, t = ut & 1, u = ut >> 1;
      const int f = 64 * s + 32 * (u >> 1) + 16 * t + 4 * (2 * (u & 1) + g1) + qq, d = 80 * dg + 16 * n + 8 * e;
      dst[c] = *(const u32x4*)(a.w2 + (size_t)f * D + d);
    }
    u32x4* z = (u32x4*)(a.ws + WS_AG); const u32x4 zero = {0, 0, 0, 0};
    for (int c = t2; c < (int)((WS_END - WS_AG) / 16); c += 32768) z[c] = zero;
  }
}

static constexpr int K1_STAGE = 53504, K1_LDS = 3 * K1_STAGE, K1_WOFF = 32768, K1_W3 = 10256;

template <bool SSM>
__device__ __forceinline__ void k1_loader(const Args& a, LAS char* lds, int lw, int lane, int rb, int cg) {
  constexpr int NW = SSM ? 4 : 5;
  unsigned xo[8], wo[NW];
  const char* xb = a.ws + WS_XN + (size_t)rb * 256 * 1280;
#pragma unroll
  for (int j = 0; j < 8; ++j) { const int rho = (8 * lw + j) * 8 + (lane >> 3); int tok = rho;
    if (SSM) { const int i = rho & 15, bb = (rho >> 4) & 3; tok = (rho & ~63) + 16 * (i >> 2) + 8 * (i & 1) + 2 * bb + ((i >> 1) & 1); }
    xo[j] = tok * 1280 + (((lane & 7) ^ (rho & 7)) << 4); }
  const char* wb; int wstep;
  if (SSM) { wb = (const char*)(lw < 2 ? a.bre : a.bim) + (size_t)cg * 64 * 1280; wstep = 128;
#pragma unroll
    for (int j = 0; j < NW; ++j) { const int rho = ((4 * lw + j) & 7) * 8 + (lane >> 3); wo[j] = rho * 1280 + (((lane & 7) ^ (rho & 7)) << 4); }
  } else { wb = (const char*)(lw < 2 ? a.w1 : a.w3) + cg * 160; wstep = 64 * F * 2;
    const int cmax = (F - cg * 80) - 8;
#pragma unroll
    for (int j = 0; j < NW; ++j) { const int c = ((5 * lw + j) % 10) * 64 + lane; const int e = c & 1, qq = (c >> 1) & 3, g1 = (c >> 3) & 1, r = c >> 4, n = r % 5, ut = r / 5, t = ut & 1, u = ut >> 1;
      const int k = 16 * u + 8 * g1 + 4 * t + qq; int col = 16 * n + 8 * e; col = col < cmax ? col : cmax; wo[j] = k * (F * 2) + col * 2; }
  }
  const int wdst = SSM ? K1_WOFF + lw * 4096 : K1_WOFF + (lw & 1) * 5120 + (lw >> 1) * K1_W3;
  auto issue = [&](int s) { LAS char* st = lds + (s % 3) * K1_STAGE; const char* xs = xb + s * 128; const char* wsrc = wb + (size_t)s * wstep;
#pragma unroll
    for (int j = 0; j < 8; ++j) glds16(xs + xo[j], st + (8 * lw + j) * 1024);
#pragma unroll
    for (int j = 0; j < NW; ++j) glds16(wsrc + wo[j], st + wdst + j * 1024); };
  ST_DECL(); ST_RT(62); ST();
  issue(0); issue(1);
  ST();
  if (SSM) WAIT_V(12); else WAIT_V(13);
  ST();
  BAR();
  issue(2);
#pragma unroll 1
  for (int t = 0; t < 10; ++t) {
    if (t + 2 < 10) { if (SSM) WAIT_V(12); else WAIT_V(13); } else WAIT_V(0);
    ST();
    BAR();
    BAR();
    ST();
    if (t + 3 < 10) issue(t + 3);
    ST();
  }
  ST_RT(63); ST_OUT(1, 8 + lw);
}

__device__ __forceinline__ f32x2 cmul_x(f32x2 lr, f32x2 li, f32x2 hr, f32x2 hi, f32x2 ur) { return lr * hr - li * hi + ur; }

template <bool SSM>
__device__ __forceinline__ void k1_compute(const Args& a, LAS char* lds, int wid, int lane, int rb, int cg) {
  const int wm = wid & 3, wn = wid >> 2, i16 = lane & 15, g = lane >> 4;
  constexpr int NC = SSM ? 4 : 5;
  f32x4 acc[4][NC];
#pragma unroll
  for (int b = 0; b < 4; ++b)
#pragma unroll
    for (int c = 0; c < NC; ++c) acc[b][c] = (f32x4){0.f, 0.f, 0.f, 0.f};
  const int xo0 = (64 * wm + i16) * 128 + ((g ^ (lane & 7)) << 4);
  int wo0, woM = 0;
  if (SSM) wo0 = K1_WOFF + (32 * wn + i16) * 128 + ((g ^ (lane & 7)) << 4);
  else { wo0 = K1_WOFF + (lane >> 5) * 2560 + (lane & 31) * 8 + wn * 768; const int p = lane & 3;
    woM = K1_WOFF + (lane >> 5) * 2560 + 512 + ((lane & 31) >> 2) * 32 + (p < 2 ? (2 * wn + p) * 8 : K1_W3 + (2 * wn + p - 2) * 8); }
  bf16x8 X0[4], W0[NC], X1[4], W1[NC];
  auto rd = [&](bf16x8 (&X)[4], bf16x8 (&W)[NC], int s, int h) { const LAS char* st = lds + (s % 3) * K1_STAGE;
    const LAS char* xp = st + (xo0 ^ (h * 64));
    if (SSM) { const LAS char* wp = st + (wo0 ^ (h * 64));
      W[0] = ld_row(wp); W[1] = ld_row(wp + 2048); W[2] = ld_row(wp + 8192); W[3] = ld_row(wp + 8192 + 2048);
#pragma unroll
      for (int b = 0; b < 4; ++b) X[b] = ld_row(xp + b * 2048);
    } else { const LAS char* wp = st + wo0 + h * 5120; const LAS char* mp = st + woM + h * 5120;
#pragma unroll
      for (int b = 0; b < 4; ++b) X[b] = ld_row(xp + b * 2048);
      W[0] = ld_tr(wp, 1280); W[1] = ld_tr(wp + 256, 1280); W[2] = ld_tr(wp + K1_W3, 1280); W[3] = ld_tr(wp + K1_W3 + 256, 1280); W[NC - 1] = ld_tr(mp, 1280); } };
  auto mm = [&](const bf16x8 (&X)[4], const bf16x8 (&W)[NC]) {
#pragma unroll
    for (int c = 0; c < NC; ++c)
#pragma unroll
      for (int b = 0; b < 4; ++b) acc[b][c] = SSM ? MFMA16(X[b], W[c], acc[b][c]) : MFMA16(W[c], X[b], acc[b][c]); };
  ST_DECL(); ST_RT(62); ST();
  BAR();
  ST();
  rd(X0, W0, 0, 0);
  SCHED();
#pragma unroll 1
  for (int t = 0; t < 9; ++t) {
    rd(X1, W1, t, 1);
    mm(X0, W0);
    ILV(SSM ? 8 : 14, SSM ? 8 : 6);
    BAR_S();
    ST();
    rd(X0, W0, t + 1, 0);
    mm(X1, W1);
    ILV(SSM ? 8 : 14, SSM ? 8 : 6);
    BAR_L();
    ST();
  }
  rd(X1, W1, 9, 1);
  mm(X0, W0);
  ILV(SSM ? 8 : 14, SSM ? 8 : 6);
  BAR_S();
  ST();
  mm(X1, W1);
  BAR_L();
  ST();
  if constexpr (!SSM) {
    char* hm = a.ws + WS_HM;
#pragma unroll
    for (int b = 0; b < 4; ++b) {
      const int tok = rb * 256 + 64 * wm + 16 * b + i16; const int t7 = tok & 7;
      char* rowp = hm + (size_t)(tok >> 7) * (27 * 16384) + (tok & 127) * 128;
#pragma unroll
      for (int j = 0; j < 2; ++j) {
        const int T = 5 * cg + 3 * wn + j;
        if (T * 16 < F) {
          const f32x4 gt = acc[b][j], up = acc[b][2 + j];
          const unsigned g01 = pk_bf16(gt[0], gt[1]), g23 = pk_bf16(gt[2], gt[3]), u01 = pk_bf16(up[0], up[1]), u23 = pk_bf16(up[2], up[3]);
          const unsigned s01 = pk_bf16(silu_f(bflo(g01)), silu_f(bfhi(g01))), s23 = pk_bf16(silu_f(bflo(g23)), silu_f(bfhi(g23)));
          u32x2 o; o.x = pk_bf16(bflo(s01) * bflo(u01), bfhi(s01) * bfhi(u01)); o.y = pk_bf16(bflo(s23) * bflo(u23), bfhi(s23) * bfhi(u23));
          const int m = T >> 1;
          *(u32x2*)(rowp + (m >> 1) * 16384 + ((((m & 1) * 4 + g) ^ t7) << 4) + (T & 1) * 8) = o;
        }
      }
      { const int T = 5 * cg + 2;
        if (T * 16 < F) {
          f32x4 v = acc[b][4];
          auto r02 = __builtin_amdgcn_permlane32_swap(__float_as_uint(v[0]), __float_as_uint(v[2]), false, false);
          auto r13 = __builtin_amdgcn_permlane32_swap(__float_as_uint(v[1]), __float_as_uint(v[3]), false, false);
          const unsigned gg = pk_bf16(__uint_as_float(r02[0]), __uint_as_float(r13[0])), uu = pk_bf16(__uint_as_float(r02[1]), __uint_as_float(r13[1]));
          const unsigned ss = pk_bf16(silu_f(bflo(gg)), silu_f(bfhi(gg)));
          const unsigned o = pk_bf16(bflo(ss) * bflo(uu), bfhi(ss) * bfhi(uu));
          const int m = T >> 1, G = 2 * wn + (g & 1);
          *(unsigned*)(rowp + (m >> 1) * 16384 + ((((m & 1) * 4 + G) ^ t7) << 4) + (T & 1) * 8 + (lane >> 5) * 4) = o;
        } }
    }
  } else {
    float lr[2], li[2], cr[2], ci[2];
#pragma unroll
    for (int n = 0; n < 2; ++n) { const int p = cg * 64 + 32 * wn + 16 * n + i16;
      const float dt = rbf(expf(bf2f(a.ldt[p]))), ar = bf2f(a.lre[p]), ai = bf2f(a.lim[p]);
      const float e = expf(dt * ar); float sn, cs; sincosf(dt * ai, &sn, &cs);
      lr[n] = e * cs; li[n] = e * sn;
      const float nr = lr[n] - 1.0f, ni = li[n], den = 1.0f / (ar * ar + ai * ai);
      cr[n] = (nr * ar + ni * ai) * den; ci[n] = (ni * ar - nr * ai) * den; }
    float er[2], ei[2], ear[2], eai[2], l8r[2], l8i[2], l16r[2], l16i[2];
    LAS f32x2* segE = (LAS f32x2*)lds;
    LAS float* graw = (LAS float*)(lds + 8192);
#pragma unroll
    for (int n = 0; n < 2; ++n) {
      const f32x2 Lr = {lr[n], lr[n]}, Li = {li[n], li[n]};
      f32x2 hr = {0.f, 0.f}, hi = {0.f, 0.f};
#pragma unroll
      for (int b = 0; b < 4; ++b)
#pragma unroll
        for (int q = 0; q < 2; ++q) { const f32x2 ur = {acc[b][n][2 * q], acc[b][n][2 * q + 1]}, ui = {acc[b][2 + n][2 * q], acc[b][2 + n][2 * q + 1]};
          const f32x2 nr = Lr * hr - Li * hi + ur, ni = Lr * hi + Li * hr + ui; hr = nr; hi = ni; }
      float pr = lr[n], pi = li[n];
#pragma unroll
      for (int k = 0; k < 3; ++k) { const float t = pr * pr - pi * pi; pi = 2.0f * pr * pi; pr = t; }
      l8r[n] = pr; l8i[n] = pi; l16r[n] = pr * pr - pi * pi; l16i[n] = 2.0f * pr * pi;
      ear[n] = hr.x; eai[n] = hi.x;
      er[n] = pr * hr.x - pi * hi.x + hr.y; ei[n] = pr * hi.x + pi * hr.x + hi.y;
      segE[(4 * wm + g) * 64 + 32 * wn + 16 * n + i16] = (f32x2){er[n], ei[n]};
    }
    ST();
    __syncthreads();
    ST();
    const int sg = 4 * wm + g;
    float c_r[2], c_i[2], p_r[2], p_i[2];
#pragma unroll
    for (int n = 0; n < 2; ++n) { float xr = 0.f, xi = 0.f, qr = 1.f, qi = 0.f;
#pragma unroll
      for (int s = 0; s < 15; ++s) { const f32x2 e = segE[s * 64 + 32 * wn + 16 * n + i16];
        const float tr = l16r[n] * xr - l16i[n] * xi + e.x, ti = l16r[n] * xi + l16i[n] * xr + e.y;
        const float ur = l16r[n] * qr - l16i[n] * qi, ui = l16r[n] * qi + l16i[n] * qr;
        const bool on = s < sg; xr = on ? tr : xr; xi = on ? ti : xi; qr = on ? ur : qr; qi = on ? ui : qi; }
      c_r[n] = xr; c_i[n] = xi; p_r[n] = qr; p_i[n] = qi;
      if (sg == 15) { const float Ar = l16r[n] * xr - l16i[n] * xi + er[n], Ai = l16r[n] * xi + l16i[n] * xr + ei[n];
        gu64* gp = (gu64*)(a.ws + WS_AG) + ((size_t)cg * 8 + rb) * 128 + (32 * wn + 16 * n + i16) * 2;
        __hip_atomic_store(gp, (1ull << 32) | __float_as_uint(Ar), __ATOMIC_RELAXED, __HIP_MEMORY_SCOPE_AGENT);
        __hip_atomic_store(gp + 1, (1ull << 32) | __float_as_uint(Ai), __ATOMIC_RELAXED, __HIP_MEMORY_SCOPE_AGENT); } }
    { const int tid = wid * 64 + lane;
#pragma unroll
      for (int q = 0; q < 2; ++q) { const int rp = (tid >> 7) + 4 * q;
        if (rp < rb) { gu64* gp = (gu64*)(a.ws + WS_AG) + ((size_t)cg * 8 + rp) * 128 + (tid & 127); unsigned long long v; unsigned spins = 0;
          do { v = __hip_atomic_load(gp, __ATOMIC_RELAXED, __HIP_MEMORY_SCOPE_AGENT); if ((v >> 32) == 1ull) break; __builtin_amdgcn_s_sleep(1); } while (++spins < (1u << 22));
          graw[rp * 128 + (tid & 127)] = __uint_as_float((unsigned)v); } } }
    ST();
    __syncthreads();
    ST();
    char* hT = a.ws + WS_HT;
#pragma unroll
    for (int n = 0; n < 2; ++n) {
      float zr = l16r[n], zi = l16i[n];
#pragma unroll
      for (int k = 0; k < 4; ++k) { const float t = zr * zr - zi * zi; zi = 2.0f * zr * zi; zr = t; }
      float Gr = 0.f, Gi = 0.f; const int ch = 32 * wn + 16 * n + i16;
      for (int rp = 0; rp < rb; ++rp) { const float ar = graw[rp * 128 + ch * 2], ai = graw[rp * 128 + ch * 2 + 1]; const float t = zr * Gr - zi * Gi + ar; Gi = zr * Gi + zi * Gr + ai; Gr = t; }
      const float inr = c_r[n] + p_r[n] * Gr - p_i[n] * Gi, ini = c_i[n] + p_r[n] * Gi + p_i[n] * Gr;
      const f32x2 Lr = {lr[n], lr[n]}, Li = {li[n], li[n]};
      f32x2 hr = {inr, l8r[n] * inr - l8i[n] * ini + ear[n]}, hi = {ini, l8r[n] * ini + l8i[n] * inr + eai[n]};
      const f32x2 Cr = {2.0f * cr[n], 2.0f * cr[n]}, Ci = {2.0f * ci[n], 2.0f * ci[n]};
      u32x4 reA, reB, imA, imB;
#pragma unroll
      for (int b = 0; b < 4; ++b) { f32x2 vr[2], vi[2];
#pragma unroll
        for (int q = 0; q < 2; ++q) { const f32x2 ur = {acc[b][n][2 * q], acc[b][n][2 * q + 1]}, ui = {acc[b][2 + n][2 * q], acc[b][2 + n][2 * q + 1]};
          const f32x2 nr = Lr * hr - Li * hi + ur, ni = Lr * hi + Li * hr + ui; hr = nr; hi = ni;
          vr[q] = Cr * hr - Ci * hi; vi[q] = -(Cr * hi + Ci * hr); }
        reA[b] = pk_bf16(vr[0].x, vr[1].x); reB[b] = pk_bf16(vr[0].y, vr[1].y); imA[b] = pk_bf16(vi[0].x, vi[1].x); imB[b] = pk_bf16(vi[0].y, vi[1].y); }
      const int u = 2 * wn + n, g1 = (i16 >> 3) & 1, t = (i16 >> 2) & 1, qq = i16 & 3, nn = 4 * (wm & 1) + g;
      char* bp = hT + (size_t)(2 * rb + (wm >> 1)) * (20 * 16384) + (((u * 2 + t) * 8 + nn) * 256) + g1 * 128 + qq * 32;
      *(u32x4*)(bp + (size_t)cg * 16384) = reA; *(u32x4*)(bp + (size_t)cg * 16384 + 16) = reB;
      *(u32x4*)(bp + (size_t)(10 + cg) * 16384) = imA; *(u32x4*)(bp + (size_t)(10 + cg) * 16384 + 16) = imB;
    }
  }
  ST();
  ST(); ST_RT(63); ST_OUT(1, wid);
}

__global__ __launch_bounds__(768, 3) void k1_gemm(Args a) {
  extern __shared__ __attribute__((aligned(256))) char smem[];
  LAS char* lds = (LAS char*)smem;
  const int wid = __builtin_amdgcn_readfirstlane(threadIdx.x >> 6), lane = threadIdx.x & 63;
  const int b = blockIdx.x, xcd = b & 7, idx = b >> 3;
  const int rb = 4 * (xcd & 1) + (idx & 3), cq = xcd >> 1, ci = idx >> 2;
  const int nf = cq < 2 ? 6 : 5, f0 = cq < 2 ? 6 * cq : 12 + 5 * (cq - 2), s0 = cq < 2 ? 2 * cq : 4 + 3 * (cq - 2);
  const int cgi = ci < nf ? f0 + ci : 22 + s0 + (ci - nf);
  if (cgi < 22) { if (wid < 8) k1_compute<false>(a, lds, wid, lane, rb, cgi); else k1_loader<false>(a, lds, wid - 8, lane, rb, cgi); }
  else { if (wid < 8) k1_compute<true>(a, lds, wid, lane, rb, cgi - 22); else k1_loader<true>(a, lds, wid - 8, lane, rb, cgi - 22); }
}

static constexpr int K2_STAGE = 26624, K2_NB = 6, K2_LDS = K2_NB * K2_STAGE;

template <bool YW>
__device__ __forceinline__ void k2_loader(const Args& a, LAS char* lds, int lw, int lane, int rb, int dg) {
  constexpr int T = YW ? 20 : 27;
  const char* tb = a.ws + (YW ? WS_HT + (size_t)rb * 20 * 16384 : WS_HM + (size_t)rb * 27 * 16384) + lw * 4096 + lane * 16;
  const int nd = lw < 2 ? 3 : 2, d0 = lw < 2 ? 3 * lw : 6 + 2 * (lw - 2);
  unsigned co[3];
#pragma unroll
  for (int j = 0; j < 3; ++j) { const int piece = d0 + (j < nd ? j : 0);
    if (YW) { const int rho = piece * 8 + (lane >> 3); co[j] = (80 * dg + rho) * 1280 + (((lane & 7) ^ (rho & 7)) << 4); }
    else co[j] = piece * 1024 + lane * 16; }
  auto issue = [&](int s) { LAS char* st = lds + (s % K2_NB) * K2_STAGE; const char* ts = tb + (size_t)s * 16384;
    const char* ds = YW ? (const char*)(s < 10 ? a.cre : a.cim) + (s % 10) * 128 : a.ws + WS_W2 + ((size_t)dg * 27 + s) * 10240;
#pragma unroll
    for (int j = 0; j < 4; ++j) glds16(ts + j * 1024, st + lw * 4096 + j * 1024);
    glds16(ds + co[0], st + 16384 + d0 * 1024); glds16(ds + co[1], st + 16384 + (d0 + 1) * 1024);
    if (lw < 2) glds16(ds + co[2], st + 16384 + (d0 + 2) * 1024); };
  ST_DECL(); ST_RT(62); ST();
  issue(0); issue(1);
  ST();
  if (lw < 2) WAIT_V(7); else WAIT_V(6);
  ST();
  BAR();
#pragma unroll
  for (int s = 2; s < K2_NB; ++s) issue(s);
#pragma unroll 1
  for (int t = 0; t < T; ++t) {
    const int newer = (T - 1 < t + K2_NB - 1 ? T - 1 : t + K2_NB - 1) - (t + 1);
    if (lw < 2) { if (newer >= 4) WAIT_V(28); else if (newer == 3) WAIT_V(21); else if (newer == 2) WAIT_V(14); else if (newer == 1) WAIT_V(7); else WAIT_V(0); }
    else { if (newer >= 4) WAIT_V(24); else if (newer == 3) WAIT_V(18); else if (newer == 2) WAIT_V(12); else if (newer == 1) WAIT_V(6); else WAIT_V(0); }
    ST();
    BAR();
    ST();
    if (t + K2_NB < T) issue(t + K2_NB);
  }
  ST_RT(63); ST_OUT(2, 8 + lw);
}

template <bool YW>
__device__ __forceinline__ void k2_compute(const Args& a, LAS char* lds, int wid, int lane, int rb, int dg, int pair) {
  constexpr int T = YW ? 20 : 27;
  const int th = wid & 1, kh = wid >> 1, i16 = lane & 15, g = lane >> 4;
  f32x4 acc[4][5];
#pragma unroll
  for (int b = 0; b < 4; ++b)
#pragma unroll
    for (int c = 0; c < 5; ++c) acc[b][c] = (f32x4){0.f, 0.f, 0.f, 0.f};
  int to, dofs;
  if (YW) { to = (lane >> 5) * 4096 + (lane & 31) * 8 + kh * 8192 + th * 1024; dofs = 16384 + i16 * 128 + (((4 * kh + g) ^ (lane & 7)) << 4); }
  else { to = (64 * th + i16) * 128 + (((4 * kh + g) ^ (lane & 7)) << 4); dofs = 16384 + (lane >> 5) * 2560 + (lane & 31) * 8 + kh * 5120; }
  bf16x8 TA[4], DA[5], TB[4], DB[5];
  auto rd = [&](bf16x8 (&X)[4], bf16x8 (&W)[5], int s) { const LAS char* st = lds + (s % K2_NB) * K2_STAGE;
#pragma unroll
    for (int b = 0; b < 4; ++b) X[b] = YW ? ld_tr(st + to + b * 256, 2048) : ld_row(st + to + b * 2048);
#pragma unroll
    for (int c = 0; c < 5; ++c) W[c] = YW ? ld_row(st + dofs + c * 2048) : ld_tr(st + dofs + c * 256, 1280); };
  auto mm = [&](const bf16x8 (&X)[4], const bf16x8 (&W)[5]) {
#pragma unroll
    for (int c = 0; c < 5; ++c)
#pragma unroll
      for (int b = 0; b < 4; ++b) acc[b][c] = MFMA16(W[c], X[b], acc[b][c]); };
  ST_DECL(); ST_RT(62); ST();
  BAR();
  ST();
  rd(TA, DA, 0);
  int t = 0;
#pragma unroll 1
  for (; t + 2 < T; t += 2) {
    BAR_L(); ST(); rd(TB, DB, t + 1); mm(TA, DA); ILV(YW ? 13 : 14, YW ? 7 : 6);
    BAR_L(); ST(); rd(TA, DA, t + 2); mm(TB, DB); ILV(YW ? 13 : 14, YW ? 7 : 6);
  }
  if (T & 1) { BAR_L(); ST(); mm(TA, DA); }
  else { BAR_L(); ST(); rd(TB, DB, t + 1); mm(TA, DA); ILV(YW ? 13 : 14, YW ? 7 : 6); BAR_L(); ST(); mm(TB, DB); }
  ST();
  __syncthreads();
  { LAS f32x4* ex = (LAS f32x4*)lds;
#pragma unroll
    for (int q = 0; q < 2; ++q)
#pragma unroll
      for (int c = 0; c < 5; ++c) ex[(wid * 10 + q * 5 + c) * 64 + lane] = kh ? acc[q][c] : acc[2 + q][c];
    __syncthreads();
#pragma unroll
    for (int q = 0; q < 2; ++q)
#pragma unroll
      for (int c = 0; c < 5; ++c) { const f32x4 o = ex[((wid ^ 2) * 10 + q * 5 + c) * 64 + lane]; if (kh) acc[q][c] = acc[2 + q][c] + o; else acc[q][c] = acc[q][c] + o; } }
  ST();
  const auto yr = __builtin_amdgcn_make_buffer_rsrc(a.ws + WS_YX + (size_t)pair * 40960, 0, 40960, 0x00020000);
  gu32* flag = (gu32*)(a.ws + WS_FL + (size_t)pair * 256);
  if (YW) {
#pragma unroll
    for (int q = 0; q < 2; ++q)
#pragma unroll
      for (int c = 0; c < 5; ++c) __builtin_amdgcn_raw_buffer_store_b128(__builtin_bit_cast(u32x4, acc[q][c]), yr, ((wid * 10 + q * 5 + c) * 64 + lane) * 16, 0, 16);
    WAIT_V(0);
    __syncthreads();
    if (wid == 0 && lane == 0) __hip_atomic_store(flag, 1u, __ATOMIC_RELAXED, __HIP_MEMORY_SCOPE_AGENT);
  } else {
    u32x2 xv[2][5], nv[2][5], dv[5];
#pragma unroll
    for (int c = 0; c < 5; ++c) dv[c] = *(const u32x2*)(a.dsk + 80 * dg + 16 * c + 4 * g);
#pragma unroll
    for (int q = 0; q < 2; ++q) { const int tok = rb * 128 + 64 * th + 16 * (2 * kh + q) + i16;
#pragma unroll
      for (int c = 0; c < 5; ++c) { const size_t o = (size_t)tok * D + 80 * dg + 16 * c + 4 * g; xv[q][c] = *(const u32x2*)(a.x + o); nv[q][c] = *(const u32x2*)((const u16*)(a.ws + WS_XN) + o); } }
    { unsigned spins = 0; while (__builtin_amdgcn_readfirstlane(__hip_atomic_load(flag, __ATOMIC_RELAXED, __HIP_MEMORY_SCOPE_AGENT)) != 1u && ++spins < (1u << 22)) __builtin_amdgcn_s_sleep(1); }
    asm volatile("" ::: "memory");
    ST();
    f32x4 yv[2][5];
#pragma unroll
    for (int q = 0; q < 2; ++q)
#pragma unroll
      for (int c = 0; c < 5; ++c) yv[q][c] = __builtin_bit_cast(f32x4, __builtin_amdgcn_raw_buffer_load_b128(yr, ((wid * 10 + q * 5 + c) * 64 + lane) * 16, 0, 16));
#pragma unroll
    for (int q = 0; q < 2; ++q) { const int tok = rb * 128 + 64 * th + 16 * (2 * kh + q) + i16;
#pragma unroll
      for (int c = 0; c < 5; ++c) {
        const f32x4 o = acc[q][c];
        const unsigned o01 = pk_bf16(o[0], o[1]), o23 = pk_bf16(o[2], o[3]);
        const unsigned g01 = pk_bf16(silu_f(bflo(o01)), silu_f(bfhi(o01))), g23 = pk_bf16(silu_f(bflo(o23)), silu_f(bfhi(o23)));
        const unsigned s01 = pk_bf16(bflo(nv[q][c].x) * bflo(dv[c].x), bfhi(nv[q][c].x) * bfhi(dv[c].x)), s23 = pk_bf16(bflo(nv[q][c].y) * bflo(dv[c].y), bfhi(nv[q][c].y) * bfhi(dv[c].y));
        f32x4 r;
        r[0] = bflo(xv[q][c].x) + (yv[q][c][0] + bflo(s01)) * bflo(g01); r[1] = bfhi(xv[q][c].x) + (yv[q][c][1] + bfhi(s01)) * bfhi(g01);
        r[2] = bflo(xv[q][c].y) + (yv[q][c][2] + bflo(s23)) * bflo(g23); r[3] = bfhi(xv[q][c].y) + (yv[q][c][3] + bfhi(s23)) * bfhi(g23);
        *(f32x4*)(a.out + (size_t)tok * D + 80 * dg + 16 * c + 4 * g) = r; } }
  }
  ST();
  ST(); ST_RT(63); ST_OUT(2, wid);
}

__global__ __launch_bounds__(512, 2) void k2_gemm(Args a) {
  extern __shared__ __attribute__((aligned(256))) char smem[];
  LAS char* lds = (LAS char*)smem;
  const int wid = __builtin_amdgcn_readfirstlane(threadIdx.x >> 6), lane = threadIdx.x & 63;
  const int b = blockIdx.x, xcd = b & 7, idx = b >> 3;
  const bool yw = xcd >= 4; const int rb = 4 * (xcd & 3) + (idx & 3), dg = idx >> 2, pair = rb * 8 + dg;
  if (yw) { if (wid < 4) k2_compute<true>(a, lds, wid, lane, rb, dg, pair); else k2_loader<true>(a, lds, wid - 4, lane, rb, dg); }
  else { if (wid < 4) k2_compute<false>(a, lds, wid, lane, rb, dg, pair); else k2_loader<false>(a, lds, wid - 4, lane, rb, dg); }
}

extern "C" void kernel_launch(void* const* d_in, const int* in_sizes, int n_in, void* d_out, int out_size, void* d_ws, size_t ws_size, hipStream_t stream) {
  Args a;
  a.x = (const u16*)d_in[0]; a.lre = (const u16*)d_in[1]; a.lim = (const u16*)d_in[2]; a.bre = (const u16*)d_in[3]; a.bim = (const u16*)d_in[4];
  a.cre = (const u16*)d_in[5]; a.cim = (const u16*)d_in[6]; a.ldt = (const u16*)d_in[7]; a.dsk = (const u16*)d_in[8];
  a.w1 = (const u16*)d_in[10]; a.w3 = (const u16*)d_in[11]; a.w2 = (const u16*)d_in[12]; a.out = (float*)d_out; a.ws = (char*)d_ws;
  (void)hipFuncSetAttribute((const void*)k1_gemm, hipFuncAttributeMaxDynamicSharedMemorySize, K1_LDS);
  (void)hipFuncSetAttribute((const void*)k2_gemm, hipFuncAttributeMaxDynamicSharedMemorySize, K2_LDS);
  k0_prep<<<256, 256, 0, stream>>>(a);
  k1_gemm<<<256, 768, K1_LDS, stream>>>(a);
  k2_gemm<<<256, 512, K2_LDS, stream>>>(a);
}
```

```cpp
#include <hip/hip_runtime.h>
#include <hip/hip_bf16.h>

typedef unsigned short u16;
typedef short bf16x8 __attribute__((ext_vector_type(8)));
typedef short s16x4 __attribute__((ext_vector_type(4)));
typedef float f32x4 __attribute__((ext_vector_type(4)));
typedef float f32x2 __attribute__((ext_vector_type(2)));
typedef unsigned u32x4 __attribute__((ext_vector_type(4)));
typedef unsigned u32x2 __attribute__((ext_vector_type(2)));
typedef __bf16 bf16x2_t __attribute__((ext_vector_type(2)));
#define LAS __attribute__((address_space(3)))
#define GAS __attribute__((address_space(1)))
typedef GAS unsigned gu32;
typedef GAS unsigned long long gu64;

static constexpr int L = 2048, D = 640, P = 640, F = 1728;
static constexpr size_t WS_HM = 0;
static constexpr size_t WS_HT = WS_HM + (size_t)16 * 27 * 16384;
static constexpr size_t WS_W2 = WS_HT + (size_t)16 * 20 * 16384;
static constexpr size_t WS_YX = WS_W2 + (size_t)8 * 27 * 10240;
static constexpr size_t WS_RS = WS_YX + (size_t)128 * 40960;
static constexpr size_t WS_AG = WS_RS + (size_t)L * 16;
static constexpr size_t WS_EP = WS_AG + (size_t)10 * 8 * 192 * 8;
static constexpr size_t WS_FL = WS_EP + (size_t)18 * 256;
static constexpr size_t WS_END = WS_FL + (size_t)128 * 256;

struct Args {
  const u16* x; const u16* lre; const u16* lim; const u16* bre; const u16* bim; const u16* cre; const u16* cim;
  const u16* ldt; const u16* dsk; const u16* w1; const u16* w3; const u16* w2; float* out; char* ws;
};

__device__ __forceinline__ float bf2f(u16 b) { return __uint_as_float(((unsigned)b) << 16); }
__device__ __forceinline__ unsigned pk_bf16(float lo, float hi) { f32x2 v = {lo, hi}; bf16x2_t r = __builtin_convertvector(v, bf16x2_t); return __builtin_bit_cast(unsigned, r); }
__device__ __forceinline__ float bflo(unsigned p) { return __uint_as_float(p << 16); }
__device__ __forceinline__ float bfhi(unsigned p) { return __uint_as_float(p & 0xffff0000u); }
__device__ __forceinline__ float rbf(float f) { return bflo(pk_bf16(f, 0.f)); }
__device__ __forceinline__ float dot2(unsigned a, unsigned b, float c) { return __builtin_amdgcn_fdot2_f32_bf16(__builtin_bit_cast(bf16x2_t, a), __builtin_bit_cast(bf16x2_t, b), c, false); }
__device__ __forceinline__ f32x2 silu2(f32x2 v) { const f32x2 t = v * -1.44269504089f; f32x2 e; e.x = __builtin_amdgcn_exp2f(t.x); e.y = __builtin_amdgcn_exp2f(t.y);
  const f32x2 d = e + 1.0f; f32x2 r; r.x = __builtin_amdgcn_rcpf(d.x); r.y = __builtin_amdgcn_rcpf(d.y); return v * r; }

#define SCHED() __builtin_amdgcn_sched_barrier(0)
#define BAR() asm volatile("s_barrier" ::: "memory")
#define BAR_L() do { SCHED(); asm volatile("s_waitcnt lgkmcnt(0)\n\ts_barrier" ::: "memory"); SCHED(); } while (0)
template <int N> __device__ __forceinline__ void wait_v() { asm volatile("s_waitcnt vmcnt(%0)" :: "i"(N) : "memory"); }
#define DSPER 1
template <int ND, int NM> __device__ __forceinline__ void ilv() {
#pragma unroll
  for (int i = 0; i < (ND + DSPER - 1) / DSPER; ++i) { __builtin_amdgcn_sched_group_barrier(0x8, 1, 0); __builtin_amdgcn_sched_group_barrier(0x100, DSPER, 0); }
  __builtin_amdgcn_sched_group_barrier(0x8, ND + NM - (ND + DSPER - 1) / DSPER, 0); }
#define MFMA16(a, b, c) __builtin_amdgcn_mfma_f32_16x16x32_bf16(a, b, c, 0, 0, 0)
__device__ __forceinline__ void glds16(const void* g, LAS char* l) { __builtin_amdgcn_global_load_lds((const unsigned*)g, (LAS unsigned*)l, 16, 0, 0); }
__device__ __forceinline__ bf16x8 ld_row(const LAS char* p) { return *(const LAS bf16x8*)p; }
typedef short v4i16_t __attribute__((ext_vector_type(4)));
__device__ __forceinline__ s16x4 ld_tr4(const LAS char* p) { return __builtin_amdgcn_ds_read_tr16_b64_v4i16((LAS v4i16_t*)p); }
__device__ __forceinline__ bf16x8 ld_tr(const LAS char* p, int t1off) { const s16x4 lo = ld_tr4(p), hi = ld_tr4(p + t1off); return __builtin_shufflevector(lo, hi, 0, 1, 2, 3, 4, 5, 6, 7); }
__device__ __forceinline__ void st16_wt(__amdgpu_buffer_rsrc_t r, unsigned off, u32x4 v) { __builtin_amdgcn_raw_buffer_store_b128(v, r, off, 0, 16); }
__device__ __forceinline__ void st8_wt(__amdgpu_buffer_rsrc_t r, unsigned off, u32x2 v) { __builtin_amdgcn_raw_buffer_store_b64(v, r, off, 0, 16); }
__device__ __forceinline__ void st4_wt(__amdgpu_buffer_rsrc_t r, unsigned off, unsigned v) { __builtin_amdgcn_raw_buffer_store_b32(v, r, off, 0, 16); }
__device__ __forceinline__ __amdgpu_buffer_rsrc_t mk_rsrc(const void* p, unsigned bytes) { return __builtin_amdgcn_make_buffer_rsrc((void*)p, 0, bytes, 0x00020000); }

#define ST_DECL()
#define ST()
#define STL()
#define ST_RT(slot)
#define ST_OUT(kern, wid_)

static constexpr int XSLOT = 32768, K1_WOFF = 3 * XSLOT, WSLOT = 10256, W3OFF = 5136, BSLOT = 16384, K1_RS = K1_WOFF + 6 * WSLOT  , EP_CS = K1_RS + 2048  , K1_LDS = EP_CS + 640;
static constexpr int SSM_TAB = K1_WOFF + 3 * BSLOT;
static constexpr int EP_W = 4096  , EP_G = 8192  ;

__device__ __forceinline__ unsigned long long mix64(unsigned long long z) { z += 0x9E3779B97F4A7C15ull; z = (z ^ (z >> 30)) * 0xBF58476D1CE4E5B9ull; z = (z ^ (z >> 27)) * 0x94D049BB133111EBull; return z ^ (z >> 31); }
__device__ __forceinline__ unsigned long long ld_dev(const void* p) { return __hip_atomic_load((gu64*)p, __ATOMIC_RELAXED, __HIP_MEMORY_SCOPE_AGENT); }
__device__ __forceinline__ void st_dev(void* p, unsigned long long v) { __hip_atomic_store((gu64*)p, v, __ATOMIC_RELAXED, __HIP_MEMORY_SCOPE_AGENT); }
__device__ __forceinline__ int ssm_token(int rho) { const int i = rho & 15, bb = (rho >> 4) & 3; return (rho & ~63) + 16 * (i >> 2) + 8 * (i & 1) + 2 * bb + ((i >> 1) & 1); }

__device__ __forceinline__ unsigned long long ld_sc(const void* p) { unsigned long long v; asm volatile("s_load_dwordx2 %0, %1, 0x0\n\ts_waitcnt lgkmcnt(0)" : "=s"(v) : "s"(p) : "memory"); return v; }
template <int C> __device__ __forceinline__ float dpp_add(float x) { return x + __int_as_float(__builtin_amdgcn_update_dpp(0, __float_as_int(x), C, 0xf, 0xf, true)); }
__device__ __forceinline__ float wave_sum(float x) {
  x = dpp_add<0xB1>(x); x = dpp_add<0x4E>(x); x = dpp_add<0x141>(x); x = dpp_add<0x140>(x);
  const int xi = __float_as_int(x);
  return (__int_as_float(__builtin_amdgcn_readlane(xi, 0)) + __int_as_float(__builtin_amdgcn_readlane(xi, 16))) + (__int_as_float(__builtin_amdgcn_readlane(xi, 32)) + __int_as_float(__builtin_amdgcn_readlane(xi, 48))); }

template <bool SSM>
__device__ __forceinline__ void k1_loader(const Args& a, LAS char* lds, int lw, int lane, int rb, int cg) {
  ST_DECL(); ST_RT(62); ST();
  unsigned xo[4];
  const char* xb = (const char*)a.x + (size_t)rb * 256 * 1280;
#pragma unroll
  for (int j = 0; j < 4; ++j) { const int rho = (4 * lw + j) * 8 + (lane >> 3); const int tok = ssm_token(rho); xo[j] = tok * 1280 + (((lane & 7) ^ (rho & 7)) << 4); }
  unsigned wo[3]; const char* wb = nullptr; const int nw = (lw & 1) ? 2 : 3;
  if (SSM) {
#pragma unroll
    for (int j = 0; j < 2; ++j) { const int rho = (2 * lw + j) * 8 + (lane >> 3); wo[j] = rho * 1280 + (((lane & 7) ^ (rho & 7)) << 4); }
    wo[2] = 0;
  } else { wb = (const char*)(lw < 2 ? a.w1 : a.w3) + cg * 160;
    const int cmax = (F - cg * 80) - 8;
#pragma unroll
    for (int j = 0; j < 3; ++j) { const int c = ((lw & 1) * 3 + (j < nw ? j : 0)) * 64 + lane; const int e = c & 1, qq = (c >> 1) & 3, g1 = (c >> 3) & 1, r = c >> 4, n = r % 5, ut = r / 5, t = ut & 1, u2 = ut >> 1;
      const int k = 16 * u2 + 8 * g1 + 4 * t + qq; int col = 16 * n + 8 * e; col = col < cmax ? col : cmax; wo[j] = k * (F * 2) + col * 2; }
  }
  const char* bre = (const char*)a.bre + (size_t)cg * 64 * 1280; const char* bim = (const char*)a.bim + (size_t)cg * 64 * 1280;
  const int wdst = K1_WOFF + (lw >> 1) * W3OFF + (lw & 1) * 3072;
  auto issue_w = [&](int u) { if (!SSM) { LAS char* st = lds + wdst + (u % 6) * WSLOT; const char* src = wb + (size_t)u * (32 * F * 2);
      glds16(src + wo[0], st); glds16(src + wo[1], st + 1024); if (!(lw & 1)) glds16(src + wo[2], st + 2048); } };
  auto issue_x = [&](int j) { const int sl = (j + 5) >> 1, part = (j + 1) & 1; LAS char* st = lds + (sl % 3) * XSLOT + part * 16384 + lw * 4096; const char* src = xb + part * (128 * 1280) + sl * 128;
#pragma unroll
    for (int q = 0; q < 4; ++q) glds16(src + xo[q], st + q * 1024);
    if (SSM) { LAS char* sb = lds + K1_WOFF + (sl % 3) * BSLOT + part * 8192 + lw * 2048; const char* sr = (part ? bim : bre) + sl * 128; glds16(sr + wo[0], sb); glds16(sr + wo[1], sb + 1024); } };
#define WAITG(NWI, NXI, EX) do { if (SSM) wait_v<(NXI) * 6 + (EX)>(); else if (lw & 1) wait_v<(NWI) * 2 + (NXI) * 4 + (EX)>(); else wait_v<(NWI) * 3 + (NXI) * 4 + (EX)>(); } while (0)
  unsigned t_dt = 0, t_re = 0, t_im = 0;
  if (SSM && lw == 0) { const int p = cg * 64 + lane;
    asm volatile("global_load_ushort %0, %3, off\n\tglobal_load_ushort %1, %4, off\n\tglobal_load_ushort %2, %5, off" : "=&v"(t_dt), "=&v"(t_re), "=&v"(t_im) : "v"(a.ldt + p), "v"(a.lre + p), "v"(a.lim + p) : "memory"); }
  issue_w(0); issue_x(-5); issue_x(-4); issue_w(1);
  const unsigned long long epoch = mix64(ld_sc(a.ws + WS_EP + (size_t)(10 + rb) * 256));
  if (SSM && lw == 0) {
    asm volatile("s_waitcnt vmcnt(12)" : "+v"(t_dt), "+v"(t_re), "+v"(t_im) :: "memory");
    const float dt = rbf(expf(bflo(t_dt))), ar = bflo(t_re), ai = bflo(t_im);
    const float e = expf(dt * ar); float sn, cs; sincosf(dt * ai, &sn, &cs);
    float pr = e * cs, pi = e * sn;
    const float nr = pr - 1.0f, ni = pi, den = 2.0f / (ar * ar + ai * ai);
    float tb[16];
    tb[0] = pr; tb[1] = pi; tb[2] = (nr * ar + ni * ai) * den; tb[3] = (ni * ar - nr * ai) * den;
#define CSQ() do { const float t_ = pr * pr - pi * pi; pi = 2.0f * pr * pi; pr = t_; } while (0)
    CSQ(); CSQ(); CSQ(); tb[4] = pr; tb[5] = pi;
    CSQ(); tb[6] = pr; tb[7] = pi;
    CSQ(); tb[8] = pr; tb[9] = pi;
    tb[10] = pr * tb[6] - pi * tb[7]; tb[11] = pr * tb[7] + pi * tb[6];
    CSQ(); tb[12] = pr; tb[13] = pi;
    CSQ(); CSQ(); tb[14] = pr; tb[15] = pi;
    const unsigned tp = (unsigned)(uintptr_t)lds + SSM_TAB + lane * 8;
#pragma unroll
    for (int q = 0; q < 8; ++q) { const f32x2 v2 = {tb[2 * q], tb[2 * q + 1]}; asm volatile("ds_write_b64 %0, %1 offset:%2" :: "v"(tp), "v"(v2), "i"(q * 512) : "memory"); }
  }
  const bf16x8 ones = {0x3f80, 0x3f80, 0x3f80, 0x3f80, 0x3f80, 0x3f80, 0x3f80, 0x3f80};
  f32x4 cs[3] = {{0.f, 0.f, 0.f, 0.f}, {0.f, 0.f, 0.f, 0.f}, {0.f, 0.f, 0.f, 0.f}};
  const unsigned lds0 = (unsigned)(uintptr_t)lds;
  const unsigned cbase = SSM ? lds0 + K1_WOFF + (32 * lw + (lane & 15)) * 128 + (((lane >> 4) ^ (lane & 7)) << 4)
                             : lds0 + K1_WOFF + (lw >> 1) * W3OFF + (lane >> 5) * 2560 + (lane & 31) * 8 + (lw & 1) * 768;
  u32x2 c0, c1, c2, c3, c4, c5; u32x4 d0, d1;
  auto cs_read = [&](int u) {
    if (SSM) { const unsigned p = (cbase + ((u >> 1) % 3) * BSLOT) ^ ((u & 1) * 64);
      asm volatile("ds_read_b128 %0, %2\n\tds_read_b128 %1, %2 offset:2048" : "=&v"(d0), "=&v"(d1) : "v"(p) : "memory");
    } else { const unsigned p = cbase + (u % 6) * WSLOT;
      asm volatile("ds_read_b64_tr_b16 %0, %6\n\tds_read_b64_tr_b16 %1, %6 offset:1280\n\tds_read_b64_tr_b16 %2, %6 offset:256\n\tds_read_b64_tr_b16 %3, %6 offset:1536\n\tds_read_b64_tr_b16 %4, %6 offset:512\n\tds_read_b64_tr_b16 %5, %6 offset:1792"
                   : "=&v"(c0), "=&v"(c1), "=&v"(c2), "=&v"(c3), "=&v"(c4), "=&v"(c5) : "v"(p) : "memory"); } };
  auto cs_acc = [&]() {
    __builtin_amdgcn_s_setprio(3);
    if (SSM) { asm volatile("s_waitcnt lgkmcnt(0)" : "+v"(d0), "+v"(d1) :: "memory");
      cs[0] = MFMA16(ones, __builtin_bit_cast(bf16x8, d0), cs[0]); cs[1] = MFMA16(ones, __builtin_bit_cast(bf16x8, d1), cs[1]);
    } else { asm volatile("s_waitcnt lgkmcnt(0)" : "+v"(c0), "+v"(c1), "+v"(c2), "+v"(c3), "+v"(c4), "+v"(c5) :: "memory");
      cs[0] = MFMA16(ones, __builtin_bit_cast(bf16x8, ((u32x4){c0.x, c0.y, c1.x, c1.y})), cs[0]);
      cs[1] = MFMA16(ones, __builtin_bit_cast(bf16x8, ((u32x4){c2.x, c2.y, c3.x, c3.y})), cs[1]);
      if (!(lw & 1)) cs[2] = MFMA16(ones, __builtin_bit_cast(bf16x8, ((u32x4){c4.x, c4.y, c5.x, c5.y})), cs[2]); }
    SCHED();
    __builtin_amdgcn_s_setprio(0);
  };
#define BAR_LD() asm volatile("s_waitcnt lgkmcnt(0)\n\ts_barrier" ::: "memory")
  ST();
  wait_v<0>();
  ST();
  BAR();
  cs_read(0);
  BAR_LD();
  cs_acc(); cs_read(1);
  issue_x(-3); issue_w(2); issue_x(-2); issue_w(3);
  issue_x(-1); issue_w(4);
  WAITG(1, 1, 0);
  BAR_LD();
  cs_acc(); cs_read(2);
  issue_w(5); issue_x(0); issue_w(6); issue_x(1); issue_w(7);
  BAR_LD();
  cs_acc(); cs_read(3);
  issue_x(2); issue_w(8);
  WAITG(3, 2, 0);
  BAR_LD();
#pragma unroll 1
  for (int j = 3; j < 14; ++j) {
    cs_acc(); cs_read(j + 1);
    issue_x(j); issue_w(j + 6);
    WAITG(2, 2, 0);
    BAR_LD();
  }
  cs_acc(); cs_read(15); issue_x(14); WAITG(1, 2, 0); BAR_LD();
  cs_acc(); cs_read(16); WAITG(0, 1, 0);
  const int rho = 64 * lw + lane; const char* gp = a.ws + WS_RS + (size_t)(rb * 256 + ssm_token(rho)) * 16;
  unsigned long long g0 = ld_dev(gp), g1 = ld_dev(gp + 8);
  BAR_LD();
  cs_acc(); cs_read(17); wait_v<2>(); BAR_LD();
  cs_acc(); cs_read(18); BAR_LD();
  cs_acc(); cs_read(19);
  { const unsigned long long tg = epoch & 0xffffffffffffull; unsigned spins = 0;
    while (((g0 >> 16) != tg || (g1 >> 16) != tg) && ++spins < (1u << 22)) { __builtin_amdgcn_s_sleep(1); g0 = ld_dev(gp); g1 = ld_dev(gp + 8); }
    ((LAS f32x2*)(lds + K1_RS))[rho] = (f32x2){__uint_as_float((unsigned)g0 << 16), 1.0f / __uint_as_float((unsigned)g1 << 16)}; }
  cs_acc();
  if (SSM) { if (lane < 16) { ((LAS float*)(lds + EP_CS))[32 * lw + lane] = cs[0][0]; ((LAS float*)(lds + EP_CS))[32 * lw + 16 + lane] = cs[1][0]; } }
  else if (lane < 16) { LAS float* o = (LAS float*)(lds + EP_CS) + (lw >> 1) * 80 + (lw & 1) * 48 + lane; o[0] = cs[0][0]; o[16] = cs[1][0]; if (!(lw & 1)) o[32] = cs[2][0]; }
  BAR_LD();
  ST();
  if (!SSM) {
    const int tid = lw * 64 + lane; const auto wr = mk_rsrc(a.ws + WS_W2 + (size_t)rb * 276480, 276480);
    u32x4 v[4]; int cc_[4];
#pragma unroll
    for (int q = 0; q < 4; ++q) { int c = cg * 786 + q * 256 + tid; const bool ok = (q * 256 + tid < 786) && c < 17280; c = ok ? c : cg * 786; cc_[q] = ok ? c * 16 : 0x7ffffff0;
      const int s = c / 640, cc = c % 640, e = cc & 1, qq = (cc >> 1) & 3, g1 = (cc >> 3) & 1, r = cc >> 4, n = r % 5, ut = r / 5, t = ut & 1, u = ut >> 1;
      const int f = 64 * s + 16 * u + 8 * g1 + 4 * t + qq, d = 80 * rb + 16 * n + 8 * e;
      v[q] = *(const u32x4*)(a.w2 + (size_t)f * D + d); }
#pragma unroll
    for (int q = 0; q < 4; ++q) st16_wt(wr, cc_[q], v[q]);
    if (cg == 0 && tid < 16) __hip_atomic_store((gu32*)(a.ws + WS_FL + (size_t)(rb * 16 + tid) * 256), 0u, __ATOMIC_RELAXED, __HIP_MEMORY_SCOPE_AGENT);
  }
  ST_RT(63); ST_OUT(1, 8 + lw);
}

template <bool SSM, int WN>
__device__ __forceinline__ void k1_compute(const Args& a, LAS char* lds, int wid, int lane, int rb, int cg) {
  const int wm = wid & 3, i16 = lane & 15, g = lane >> 4; constexpr int wn = WN;
  constexpr int NC = SSM ? 4 : 5;
  ST_DECL(); ST_RT(62); ST();
  unsigned long long epoch = 0;
  {
    const int row = rb * 256 + 8 * (SSM ? 22 + cg : cg) + wid; const unsigned* xr = (const unsigned*)(a.x + (size_t)row * D);
    unsigned v[5];
#pragma unroll
    for (int q = 0; q < 5; ++q) v[q] = xr[lane + 64 * q];
    const unsigned long long er = mix64(ld_sc(a.ws + WS_EP + (size_t)(10 + rb) * 256));
    if (SSM) epoch = mix64(ld_sc(a.ws + WS_EP + (size_t)cg * 256));
    float s = 0.f;
#pragma unroll
    for (int q = 0; q < 5; ++q) s += bflo(v[q]) + bfhi(v[q]);
    const float mean = wave_sum(s) * (1.0f / D); float q2 = 0.f;
#pragma unroll
    for (int q = 0; q < 5; ++q) { const float d0 = bflo(v[q]) - mean, d1 = bfhi(v[q]) - mean; q2 += d0 * d0 + d1 * d1; }
    q2 = wave_sum(q2);
    const unsigned mb = pk_bf16(mean, 0.f) & 0xffff, sb = pk_bf16(__builtin_sqrtf(rbf(rbf(q2 * (1.0f / D)) + 1e-5f)), 0.f) & 0xffff;
    if (lane < 2) st_dev(a.ws + WS_RS + (size_t)row * 16 + lane * 8, (er << 16) | (lane ? sb : mb));
  }
  f32x4 acc[4][NC];
#pragma unroll
  for (int b = 0; b < 4; ++b)
#pragma unroll
    for (int c = 0; c < NC; ++c) acc[b][c] = (f32x4){0.f, 0.f, 0.f, 0.f};
  const int xo0 = (64 * wm + i16) * 128 + ((g ^ (lane & 7)) << 4);
  int wo0, woM = 0;
  if (SSM) wo0 = K1_WOFF + (32 * wn + i16) * 128 + ((g ^ (lane & 7)) << 4);
  else { wo0 = K1_WOFF + (lane >> 5) * 2560 + (lane & 31) * 8 + wn * 768; const int p = lane & 3;
    woM = K1_WOFF + (lane >> 5) * 2560 + 512 + ((lane & 31) >> 2) * 32 + (p < 2 ? (2 * wn + p) * 8 : W3OFF + (2 * wn + p - 2) * 8); }
  bf16x8 X0[4], W0[NC], X1[4], W1[NC];
  int xs = 0, ws = 0;
  auto rd = [&](bf16x8 (&X)[4], bf16x8 (&W)[NC], int h) {
    const LAS char* xp = lds + xs + (xo0 ^ (h * 64));
    if (SSM) { const LAS char* wp = lds + (xs >> 1) + (wo0 ^ (h * 64));
      W[0] = ld_row(wp); W[1] = ld_row(wp + 2048); W[2] = ld_row(wp + 8192); W[3] = ld_row(wp + 8192 + 2048);
#pragma unroll
      for (int b = 0; b < 4; ++b) X[b] = ld_row(xp + b * 2048);
    } else { const LAS char* wp = lds + ws + wo0; const LAS char* mp = lds + ws + woM;
#pragma unroll
      for (int b = 0; b < 4; ++b) X[b] = ld_row(xp + b * 2048);
      W[0] = ld_tr(wp, 1280); W[1] = ld_tr(wp + 256, 1280); W[2] = ld_tr(wp + W3OFF, 1280); W[3] = ld_tr(wp + W3OFF + 256, 1280); W[NC - 1] = ld_tr(mp, 1280); }
    ws = ws + WSLOT == 6 * WSLOT ? 0 : ws + WSLOT;
    if (h) xs = xs + XSLOT == 3 * XSLOT ? 0 : xs + XSLOT; };
  auto mm = [&](const bf16x8 (&X)[4], const bf16x8 (&W)[NC]) {
#pragma unroll
    for (int c = 0; c < NC; ++c)
#pragma unroll
      for (int b = 0; b < 4; ++b) acc[b][c] = MFMA16(X[b], W[c], acc[b][c]); };
  constexpr int ND = SSM ? 8 : 14, NM = SSM ? 8 : 6;
  ST();
  BAR();
  ST();
  rd(X0, W0, 0);
  SCHED();
#pragma unroll 1
  for (int j = 0; j < 20; j += 2) {
    BAR_L(); STL(); rd(X1, W1, 1); mm(X0, W0); ilv<ND, NM>();
    BAR_L(); STL(); rd(X0, W0, 0); mm(X1, W1); ilv<ND, NM>();
  }
  SCHED();
  ST();
  const int tid = wid * 64 + lane;
  if ((SSM ? 22 + cg : cg) == 0 && tid == 0) st_dev(a.ws + WS_EP + (size_t)(10 + rb) * 256, mix64(ld_dev(a.ws + WS_EP + (size_t)(10 + rb) * 256)));
  const LAS f32x2* rst = (const LAS f32x2*)(lds + K1_RS);
  if constexpr (!SSM) {
    const auto hr = mk_rsrc(a.ws + WS_HM + (size_t)(2 * rb + (wm >> 1)) * (27 * 16384), 27 * 16384);
    const LAS float* csp = (const LAS float*)(lds + EP_CS);
    const bool lo = i16 < 8;
    float ng[2], nu[2];
#pragma unroll
    for (int j = 0; j < 2; ++j) { ng[j] = -csp[16 * (3 * wn + j) + i16]; nu[j] = -csp[80 + 16 * (3 * wn + j) + i16]; }
    const float nm = -csp[(lo ? 32 : 104) + 8 * wn + i16];
    u32x4 oA[2], oB[2], oM;
#pragma unroll
    for (int b = 0; b < 4; ++b) {
      const f32x4 p0 = *(const LAS f32x4*)(rst + 64 * wm + 16 * b + 4 * g), p1 = *(const LAS f32x4*)(rst + 64 * wm + 16 * b + 4 * g + 2);
      const f32x4 m = {p0[0], p0[2], p1[0], p1[2]}, r = {p0[1], p0[3], p1[1], p1[3]}, rn = r * -1.44269504089f, r2 = r * r;
      auto act = [&](float tg, float tu, float rn_, float r2_) { return (tg * tu) * r2_ * __builtin_amdgcn_rcpf(1.0f + __builtin_amdgcn_exp2f(tg * rn_)); };
#pragma unroll
      for (int j = 0; j < 2; ++j) { const f32x4 tg = acc[b][j] + m * ng[j], tu = acc[b][2 + j] + m * nu[j];
        oA[j][b] = pk_bf16(act(tg[0], tu[0], rn[0], r2[0]), act(tg[2], tu[2], rn[2], r2[2])); oB[j][b] = pk_bf16(act(tg[1], tu[1], rn[1], r2[1]), act(tg[3], tu[3], rn[3], r2[3])); }
      { const f32x4 v = acc[b][4] + m * nm;
        const float s0 = lo ? v[1] : v[0], s1 = lo ? v[3] : v[2], a0 = lo ? v[0] : v[1], a1 = lo ? v[2] : v[3];
        const float q0 = __int_as_float(__builtin_amdgcn_update_dpp(0, __float_as_int(s0), 0x128, 0xf, 0xf, false)), q1 = __int_as_float(__builtin_amdgcn_update_dpp(0, __float_as_int(s1), 0x128, 0xf, 0xf, false));
        const float g0 = lo ? a0 : q0, g1 = lo ? a1 : q1, u0 = lo ? q0 : a0, u1 = lo ? q1 : a1;
        oM[b] = pk_bf16(act(g0, u0, lo ? rn[0] : rn[1], lo ? r2[0] : r2[1]), act(g1, u1, lo ? rn[2] : rn[3], lo ? r2[2] : r2[3])); }
    }
    const unsigned nn = 4 * (wm & 1) + g, tq = ((i16 >> 2) & 1) * 2048 + nn * 256 + (i16 & 3) * 16;
#pragma unroll
    for (int j = 0; j < 2; ++j) { const int f0 = 80 * cg + 16 * (3 * wn + j);
      if (f0 < F) { const unsigned o = (f0 >> 6) * 16384 + ((f0 >> 4) & 3) * 4096 + (i16 >> 3) * 128 + tq; st16_wt(hr, o, oA[j]); st16_wt(hr, o + 64, oB[j]); } }
    { const int f0 = 80 * cg + 32 + 8 * wn; st16_wt(hr, (f0 >> 6) * 16384 + ((f0 >> 4) & 3) * 4096 + ((f0 >> 3) & 1) * 128 + (i16 >> 3) * 64 + tq, oM); }
  } else {
    const LAS f32x2* tab = (const LAS f32x2*)(lds + SSM_TAB);
    f32x2 lam[2], l8[2];
#pragma unroll
    for (int b = 0; b < 4; ++b) { const f32x4 p0 = *(const LAS f32x4*)(rst + 64 * wm + 16 * b + 4 * g), p1 = *(const LAS f32x4*)(rst + 64 * wm + 16 * b + 4 * g + 2);
      const f32x4 m = {p0[0], p0[2], p1[0], p1[2]}, r = {p0[1], p0[3], p1[1], p1[3]};
#pragma unroll
      for (int n = 0; n < 2; ++n) { const int ch = 32 * wn + 16 * n + i16; const float csr = -((const LAS float*)(lds + EP_CS))[ch], csi = -((const LAS float*)(lds + EP_CS))[64 + ch];
        acc[b][n] = (acc[b][n] + m * csr) * r; acc[b][2 + n] = (acc[b][2 + n] + m * csi) * r; } }
    f32x2 ea[2], ex[2], inc[2];
#pragma unroll
    for (int n = 0; n < 2; ++n) { const int ch = 32 * wn + 16 * n + i16;
      lam[n] = tab[ch]; l8[n] = tab[128 + ch]; const f32x2 l16 = tab[192 + ch], l32 = tab[256 + ch];
      const f32x2 Lr = {lam[n].x, lam[n].x}, Li = {lam[n].y, lam[n].y};
      f32x2 hr = {acc[0][n][0], acc[0][n][1]}, hi = {acc[0][2 + n][0], acc[0][2 + n][1]};
#pragma unroll
      for (int s = 1; s < 8; ++s) { const int b = s >> 1, q = s & 1; const f32x2 ur = {acc[b][n][2 * q], acc[b][n][2 * q + 1]}, ui = {acc[b][2 + n][2 * q], acc[b][2 + n][2 * q + 1]};
        const f32x2 nr = Lr * hr - Li * hi + ur, ni = Lr * hi + Li * hr + ui; hr = nr; hi = ni; }
      ea[n] = (f32x2){hr.x, hi.x};
      float er = l8[n].x * hr.x - l8[n].y * hi.x + hr.y, ei = l8[n].x * hi.x + l8[n].y * hr.x + hi.y;
      { float pr = __shfl_up(er, 16), pi = __shfl_up(ei, 16); if (g < 1) { pr = 0.f; pi = 0.f; }
        er += l16.x * pr - l16.y * pi; ei += l16.x * pi + l16.y * pr; }
      { float pr = __shfl_up(er, 32), pi = __shfl_up(ei, 32); if (g < 2) { pr = 0.f; pi = 0.f; }
        er += l32.x * pr - l32.y * pi; ei += l32.x * pi + l32.y * pr; }
      inc[n] = (f32x2){er, ei};
      { float pr = __shfl_up(er, 16), pi = __shfl_up(ei, 16); if (g < 1) { pr = 0.f; pi = 0.f; } ex[n] = (f32x2){pr, pi}; }
      if (g == 3) ((LAS f32x2*)(lds + EP_W))[wm * 64 + ch] = inc[n];
    }
    ST();
    __syncthreads();
    f32x2 cw[2], pw[2];
#pragma unroll
    for (int n = 0; n < 2; ++n) { const int ch = 32 * wn + 16 * n + i16; const f32x2 l64 = tab[384 + ch];
      float xr = 0.f, xi = 0.f, qr = 1.f, qi = 0.f;
      for (int w = 0; w < wm; ++w) { const f32x2 e = ((const LAS f32x2*)(lds + EP_W))[w * 64 + ch];
        const float t = l64.x * xr - l64.y * xi + e.x; xi = l64.x * xi + l64.y * xr + e.y; xr = t; const float u = l64.x * qr - l64.y * qi; qi = l64.x * qi + l64.y * qr; qr = u; }
      if (wm == 3 && g == 3) { const float Ar = l64.x * xr - l64.y * xi + inc[n].x, Ai = l64.x * xi + l64.y * xr + inc[n].y;
        const unsigned long long v = ((unsigned long long)__float_as_uint(Ai) << 32) | __float_as_uint(Ar), tg = epoch << 22;
        char* gp = a.ws + WS_AG + (((size_t)cg * 8 + rb) * 192 + ch * 3) * 8;
        st_dev(gp, tg | (v & 0x3fffff)); st_dev(gp + 8, tg | ((v >> 22) & 0x3fffff)); st_dev(gp + 16, tg | (v >> 44)); }
      const f32x2 lg = g == 0 ? (f32x2){1.f, 0.f} : tab[128 + 64 * g + ch];
      cw[n] = (f32x2){ex[n].x + lg.x * xr - lg.y * xi, ex[n].y + lg.x * xi + lg.y * xr};
      pw[n] = (f32x2){lg.x * qr - lg.y * qi, lg.x * qi + lg.y * qr}; }
    ST();
    if (tid < 384) { const int r0 = tid >= 192 ? 1 : 0, k = tid - 192 * r0; const char* gp = a.ws + WS_AG + (((size_t)cg * 8 + r0) * 192 + k) * 8;
      const unsigned long long tg = epoch & 0x3ffffffffffull; unsigned long long v[4]; unsigned spins = 0; bool ok;
#pragma unroll 1
      do { ok = true;
#pragma unroll
        for (int q = 0; q < 4; ++q) v[q] = (r0 + 2 * q < rb) ? ld_dev(gp + (size_t)q * (2 * 192 * 8)) : (tg << 22);
#pragma unroll
        for (int q = 0; q < 4; ++q) ok &= (v[q] >> 22) == tg;
        if (!ok) __builtin_amdgcn_s_sleep(1);
      } while (!ok && ++spins < (1u << 22));
#pragma unroll
      for (int q = 0; q < 4; ++q) ((LAS unsigned*)(lds + EP_G))[(r0 + 2 * q) * 192 + k] = (unsigned)v[q] & 0x3fffff; }
    ST();
    __syncthreads();
    if (rb == 7 && tid == 0) st_dev(a.ws + WS_EP + (size_t)cg * 256, epoch);
    ST();
    const auto tr = mk_rsrc(a.ws + WS_HT + (size_t)(2 * rb + (wm >> 1)) * (20 * 16384), 20 * 16384);
#pragma unroll
    for (int n = 0; n < 2; ++n) {
      const int ch = 32 * wn + 16 * n + i16; const f32x2 z = tab[448 + ch], cf = tab[64 + ch];
      float Gr = 0.f, Gi = 0.f;
      for (int rp = 0; rp < rb; ++rp) { const LAS unsigned* gp = (const LAS unsigned*)(lds + EP_G) + rp * 192 + ch * 3; const unsigned p0 = gp[0], p1 = gp[1], p2 = gp[2];
        const float ar = __uint_as_float(p0 | (p1 << 22)), ai = __uint_as_float((p1 >> 10) | (p2 << 12)); const float t = z.x * Gr - z.y * Gi + ar; Gi = z.x * Gi + z.y * Gr + ai; Gr = t; }
      const float inr = cw[n].x + pw[n].x * Gr - pw[n].y * Gi, ini = cw[n].y + pw[n].x * Gi + pw[n].y * Gr;
      const f32x2 Lr = {lam[n].x, lam[n].x}, Li = {lam[n].y, lam[n].y}, Cr = {cf.x, cf.x}, Ci = {cf.y, cf.y};
      f32x2 hr = {inr, l8[n].x * inr - l8[n].y * ini + ea[n].x}, hi = {ini, l8[n].x * ini + l8[n].y * inr + ea[n].y};
      u32x4 reA, reB, imA, imB;
#pragma unroll
      for (int b = 0; b < 4; ++b) { f32x2 vr[2], vi[2];
#pragma unroll
        for (int q = 0; q < 2; ++q) { const f32x2 ur = {acc[b][n][2 * q], acc[b][n][2 * q + 1]}, ui = {acc[b][2 + n][2 * q], acc[b][2 + n][2 * q + 1]};
          const f32x2 nr = Lr * hr - Li * hi + ur, ni = Lr * hi + Li * hr + ui; hr = nr; hi = ni;
          vr[q] = Cr * hr - Ci * hi; vi[q] = -(Cr * hi) - Ci * hr; }
        reA[b] = pk_bf16(vr[0].x, vr[1].x); reB[b] = pk_bf16(vr[0].y, vr[1].y); imA[b] = pk_bf16(vi[0].x, vi[1].x); imB[b] = pk_bf16(vi[0].y, vi[1].y); }
      const int u = 2 * wn + n, g1 = (i16 >> 3) & 1, t = (i16 >> 2) & 1, qq = i16 & 3, nn = 4 * (wm & 1) + g;
      const unsigned bo = (((u * 2 + t) * 8 + nn) * 256) + g1 * 128 + qq * 16 + cg * 16384;
      st16_wt(tr, bo, reA); st16_wt(tr, bo + 64, reB); st16_wt(tr, bo + 10 * 16384, imA); st16_wt(tr, bo + 10 * 16384 + 64, imB);
    }
  }
  ST();
  ST(); ST_RT(63); ST_OUT(1, wid);
}

__global__ __launch_bounds__(768, 3) void k1_gemm(Args a) {
  extern __shared__ __attribute__((aligned(256))) char smem[];
  LAS char* lds = (LAS char*)smem;
  const int wid = __builtin_amdgcn_readfirstlane(threadIdx.x >> 6), lane = threadIdx.x & 63;
  const int b = blockIdx.x, xcd = b & 7, idx = b >> 3;
  const int rb = 4 * (xcd & 1) + (idx & 3), cq = xcd >> 1, ci = idx >> 2;
  const int nf = cq < 2 ? 6 : 5, f0 = cq < 2 ? 6 * cq : 12 + 5 * (cq - 2), s0 = cq < 2 ? 2 * cq : 4 + 3 * (cq - 2);
  if (ci < nf) { const int cg = f0 + ci; if (wid < 4) k1_compute<false, 0>(a, lds, wid, lane, rb, cg); else if (wid < 8) k1_compute<false, 1>(a, lds, wid, lane, rb, cg);
 else k1_loader<false>(a, lds, wid - 8, lane, rb, cg); }
  else { const int cg = s0 + ci - nf; if (wid < 4) k1_compute<true, 0>(a, lds, wid, lane, rb, cg); else if (wid < 8) k1_compute<true, 1>(a, lds, wid, lane, rb, cg); else k1_loader<true>(a, lds, wid - 8, lane, rb, cg); }
}

static constexpr int K2_STAGE = 26624, K2_NB = 6, K2_LDS = K2_NB * K2_STAGE;

template <bool YW>
__device__ __forceinline__ void k2_loader(const Args& a, LAS char* lds, int lw, int lane, int rb, int dg) {
  constexpr int T = YW ? 20 : 27;
  ST_DECL(); ST_RT(62); ST();
  const char* tb = a.ws + (YW ? WS_HT + (size_t)rb * 20 * 16384 : WS_HM + (size_t)rb * 27 * 16384) + lane * 16;
  const unsigned co = YW ? (80 * dg + (lane >> 3)) * 1280 + (((lane & 7) ^ (lane >> 3)) << 4) : lane * 16; constexpr unsigned cstep = YW ? 8 * 1280 : 1024;
  auto dbase = [&](int s) { return YW ? (const char*)(s < 10 ? a.cre : a.cim) + (s % 10) * 128 : a.ws + WS_W2 + ((size_t)dg * 27 + s) * 10240; };
  const int d0 = lw < 2 ? 3 * lw : 6 + 2 * (lw - 2);
  auto issue = [&](int s) { LAS char* st = lds + (s % K2_NB) * K2_STAGE; const char* ts = tb + (size_t)s * 16384 + lw * 4096; const char* ds = dbase(s) + co + d0 * cstep;
    glds16(ds, st + 16384 + d0 * 1024); glds16(ds + cstep, st + 16384 + (d0 + 1) * 1024);
    if (lw < 2) glds16(ds + 2 * cstep, st + 16384 + (d0 + 2) * 1024);
#pragma unroll
    for (int j = 0; j < 4; ++j) glds16(ts + j * 1024, st + lw * 4096 + j * 1024); };
#define WAITK(k, ex) do { if (lw < 2) wait_v<(k) * 7 + (ex)>(); else wait_v<(k) * 6 + (ex)>(); } while (0)
  issue(0);
  ST();
  wait_v<0>();
  ST();
  BAR();
  issue(1); issue(2); WAITK(1, 0);
  BAR();
  issue(3); issue(4); WAITK(2, 0);
  BAR();
  issue(5); issue(6); issue(7); WAITK(4, 0);
  BAR();
#pragma unroll 1
  for (int t = 2; t < T - 6; ++t) { issue(t + K2_NB); WAITK(4, 0); BAR(); }
  WAITK(3, 0); BAR();
  if (!YW) { LAS char* st = lds + ((T - 6) % K2_NB) * K2_STAGE;
#pragma unroll
    for (int j = 0; j < 5; ++j) { const int c = (lw * 5 + j) * 64 + lane, row = c / 10, ch = c % 10; glds16((const char*)a.x + (size_t)(rb * 128 + row) * 1280 + dg * 160 + ch * 16, st + (lw * 5 + j) * 1024); }
    glds16(a.ws + WS_RS + (size_t)(rb * 128 + (lw & 1) * 64 + lane) * 16, st + 20480 + (lw & 1) * 1024); }
  WAITK(2, YW ? 0 : 6); BAR();
  WAITK(1, YW ? 0 : 6); BAR();
  WAITK(0, YW ? 0 : 6); BAR();
  wait_v<0>(); BAR();
  ST_RT(63); ST_OUT(2, 8 + lw);
}

template <bool YW>
__device__ __forceinline__ void k2_compute(const Args& a, LAS char* lds, int wid, int lane, int rb, int dg, int pair) {
  constexpr int T = YW ? 20 : 27;
  const int th = wid & 1, kh = wid >> 1, i16 = lane & 15, g = lane >> 4;
  ST_DECL(); ST_RT(62); ST();
  f32x4 acc[4][5];
#pragma unroll
  for (int b = 0; b < 4; ++b)
#pragma unroll
    for (int c = 0; c < 5; ++c) acc[b][c] = (f32x4){0.f, 0.f, 0.f, 0.f};
  const int to = (lane >> 5) * 4096 + ((lane >> 4) & 1) * 128 + ((lane >> 1) & 1) * 64 + ((lane >> 2) & 3) * 16 + (lane & 1) * 8 + kh * 8192 + th * 1024;
  const int dofs = YW ? 16384 + i16 * 128 + (((4 * kh + g) ^ (lane & 7)) << 4) : 16384 + (lane >> 5) * 2560 + (lane & 31) * 8 + kh * 5120;
  bf16x8 TA[4], DA[5], TB[4], DB[5];
  int so = 0;
  auto rd = [&](bf16x8 (&X)[4], bf16x8 (&W)[5]) { const LAS char* st = lds + so;
#pragma unroll
    for (int c = 0; c < 5; ++c) W[c] = YW ? ld_row(st + dofs + c * 2048) : ld_tr(st + dofs + c * 256, 1280);
#pragma unroll
    for (int b = 0; b < 4; ++b) X[b] = ld_tr(st + to + b * 256, 2048);
    so = so + K2_STAGE == K2_LDS ? 0 : so + K2_STAGE; };
  auto mm = [&](const bf16x8 (&X)[4], const bf16x8 (&W)[5]) {
#pragma unroll
    for (int c = 0; c < 5; ++c)
#pragma unroll
      for (int b = 0; b < 4; ++b) acc[b][c] = MFMA16(W[c], X[b], acc[b][c]); };
  constexpr int ND = YW ? 13 : 18, NM = 20 - ND;
  auto two = [&]() { BAR_L(); rd(TB, DB); mm(TA, DA); ilv<ND, NM>(); BAR_L(); rd(TA, DA); mm(TB, DB); ilv<ND, NM>(); };
  const auto yr = mk_rsrc(a.ws + WS_YX + (size_t)pair * 40960, 40960);
  gu32* flag = (gu32*)(a.ws + WS_FL + (size_t)pair * 256);
  BAR();
  ST();
  rd(TA, DA);
  SCHED();
  u32x2 dv[5]; f32x4 yv[2][5]; bool have_y = false;
  if (YW) {
#pragma unroll 1
    for (int t = 0; t < T; t += 2) two();
  } else {
#pragma unroll 1
    for (int t = 0; t < 22; t += 2) {
      two(); }
    const unsigned f0 = __hip_atomic_load(flag, __ATOMIC_RELAXED, __HIP_MEMORY_SCOPE_AGENT);
#pragma unroll
    for (int c = 0; c < 5; ++c) dv[c] = *(const u32x2*)(a.dsk + 80 * dg + 16 * c + 4 * g);
    two();
    if (__builtin_amdgcn_readfirstlane(f0) == 1u) { have_y = true;
#pragma unroll
      for (int q = 0; q < 2; ++q)
#pragma unroll
        for (int c = 0; c < 5; ++c) yv[q][c] = __builtin_bit_cast(f32x4, __builtin_amdgcn_raw_buffer_load_b128(yr, ((wid * 10 + q * 5 + c) * 64 + lane) * 16, 0, 16)); }
    two();
    BAR_L(); mm(TA, DA);
  }
  ST();
  __syncthreads();
  { LAS f32x4* ex = (LAS f32x4*)lds;
#pragma unroll
    for (int q = 0; q < 2; ++q)
#pragma unroll
      for (int c = 0; c < 5; ++c) ex[(wid * 10 + q * 5 + c) * 64 + lane] = kh ? acc[q][c] : acc[2 + q][c];
    __syncthreads();
#pragma unroll
    for (int q = 0; q < 2; ++q)
#pragma unroll
      for (int c = 0; c < 5; ++c) { const f32x4 o = ex[((wid ^ 2) * 10 + q * 5 + c) * 64 + lane]; if (kh) acc[q][c] = acc[2 + q][c] + o; else acc[q][c] = acc[q][c] + o; } }
  ST();
  if (YW) {
#pragma unroll
    for (int q = 0; q < 2; ++q)
#pragma unroll
      for (int c = 0; c < 5; ++c) st16_wt(yr, ((wid * 10 + q * 5 + c) * 64 + lane) * 16, __builtin_bit_cast(u32x4, acc[q][c]));
    wait_v<0>();
    __syncthreads();
    if (wid == 0 && lane == 0) __hip_atomic_store(flag, 1u, __ATOMIC_RELAXED, __HIP_MEMORY_SCOPE_AGENT);
  } else {
    if (!have_y) {
      unsigned spins = 0; while (__builtin_amdgcn_readfirstlane(__hip_atomic_load(flag, __ATOMIC_RELAXED, __HIP_MEMORY_SCOPE_AGENT)) != 1u && ++spins < (1u << 22)) __builtin_amdgcn_s_sleep(1);
      asm volatile("" ::: "memory");
#pragma unroll
      for (int q = 0; q < 2; ++q)
#pragma unroll
        for (int c = 0; c < 5; ++c) yv[q][c] = __builtin_bit_cast(f32x4, __builtin_amdgcn_raw_buffer_load_b128(yr, ((wid * 10 + q * 5 + c) * 64 + lane) * 16, 0, 16)); }
    ST();
    const LAS char* xt = lds + ((T - 6) % K2_NB) * K2_STAGE;
#pragma unroll
    for (int q = 0; q < 2; ++q) { const int row = 64 * th + 16 * (2 * kh + q) + i16, tok = rb * 128 + row; const u32x4 rs = *(const LAS u32x4*)(xt + 20480 + row * 16);
      const float m = __uint_as_float(rs[0] << 16), r = __builtin_amdgcn_rcpf(__uint_as_float(rs[2] << 16));
#pragma unroll
      for (int c = 0; c < 5; ++c) {
        const f32x4 o = acc[q][c]; const u32x2 xv = *(const LAS u32x2*)(xt + row * 160 + c * 32 + g * 8);
        const f32x2 g01 = silu2((f32x2){o[0], o[1]}), g23 = silu2((f32x2){o[2], o[3]});
        const f32x4 xf = {bflo(xv.x), bfhi(xv.x), bflo(xv.y), bfhi(xv.y)}, df = {bflo(dv[c].x), bfhi(dv[c].x), bflo(dv[c].y), bfhi(dv[c].y)};
        const f32x4 s = yv[q][c] + (xf - m) * r * df;
        const f32x4 o4 = xf + s * (f32x4){g01.x, g01.y, g23.x, g23.y};
        *(f32x4*)(a.out + (size_t)tok * D + 80 * dg + 16 * c + 4 * g) = o4; } }
  }
  ST();
  ST(); ST_RT(63); ST_OUT(2, wid);
}

__global__ __launch_bounds__(512, 2) void k2_gemm(Args a) {
  extern __shared__ __attribute__((aligned(256))) char smem[];
  LAS char* lds = (LAS char*)smem;
  const int wid = __builtin_amdgcn_readfirstlane(threadIdx.x >> 6), lane = threadIdx.x & 63;
  const int b = blockIdx.x, xcd = b & 7, idx = b >> 3;
  const bool yw = (idx & 1) != 0; const int rb = 4 * (xcd & 3) + ((idx >> 1) & 3), dg = 4 * (xcd >> 2) + (idx >> 3), pair = rb * 8 + dg;
  if (yw) { if (wid < 4) k2_compute<true>(a, lds, wid, lane, rb, dg, pair); else k2_loader<true>(a, lds, wid - 4, lane, rb, dg); }
  else { if (wid < 4) k2_compute<false>(a, lds, wid, lane, rb, dg, pair); else k2_loader<false>(a, lds, wid - 4, lane, rb, dg); }
}

extern "C" void kernel_launch(void* const* d_in, const int* in_sizes, int n_in, void* d_out, int out_size, void* d_ws, size_t ws_size, hipStream_t stream) {
  Args a;
  a.x = (const u16*)d_in[0]; a.lre = (const u16*)d_in[1]; a.lim = (const u16*)d_in[2]; a.bre = (const u16*)d_in[3]; a.bim = (const u16*)d_in[4];
  a.cre = (const u16*)d_in[5]; a.cim = (const u16*)d_in[6]; a.ldt = (const u16*)d_in[7]; a.dsk = (const u16*)d_in[8];
  a.w1 = (const u16*)d_in[10]; a.w3 = (const u16*)d_in[11]; a.w2 = (const u16*)d_in[12]; a.out = (float*)d_out; a.ws = (char*)d_ws;
  (void)hipFuncSetAttribute((const void*)k1_gemm, hipFuncAttributeMaxDynamicSharedMemorySize, K1_LDS);
  (void)hipFuncSetAttribute((const void*)k2_gemm, hipFuncAttributeMaxDynamicSharedMemorySize, K2_LDS);
  k1_gemm<<<256, 768, K1_LDS, stream>>>(a);
  k2_gemm<<<256, 512, K2_LDS, stream>>>(a);
}
```

```cpp
#include <hip/hip_runtime.h>
#include <hip/hip_bf16.h>

typedef unsigned short u16;
typedef short bf16x8 __attribute__((ext_vector_type(8)));
typedef short s16x4 __attribute__((ext_vector_type(4)));
typedef float f32x4 __attribute__((ext_vector_type(4)));
typedef float f32x2 __attribute__((ext_vector_type(2)));
typedef unsigned u32x4 __attribute__((ext_vector_type(4)));
typedef unsigned u32x2 __attribute__((ext_vector_type(2)));
typedef __bf16 bf16x2_t __attribute__((ext_vector_type(2)));
#define LAS __attribute__((address_space(3)))
#define GAS __attribute__((address_space(1)))
typedef GAS unsigned gu32;
typedef GAS unsigned long long gu64;

static constexpr int L = 2048, D = 640, P = 640, F = 1728;
static constexpr size_t WS_HM = 0;
static constexpr size_t WS_HT = WS_HM + (size_t)16 * 27 * 16384;
static constexpr size_t WS_W2 = WS_HT + (size_t)16 * 20 * 16384;
static constexpr size_t WS_YX = WS_W2 + (size_t)8 * 27 * 10240;
static constexpr size_t WS_RS = WS_YX + (size_t)128 * 40960;
static constexpr size_t WS_AG = WS_RS + (size_t)L * 16;
static constexpr size_t WS_EP = WS_AG + (size_t)10 * 8 * 192 * 8;
static constexpr size_t WS_DN = WS_EP + 256;
static constexpr size_t WS_FL = WS_DN + (size_t)256 * 64;
static constexpr size_t WS_END = WS_FL + (size_t)512 * 64;
static constexpr unsigned SPIN_MAX = 1u << 18;

struct Args {
  const u16* x; const u16* lre; const u16* lim; const u16* bre; const u16* bim; const u16* cre; const u16* cim;
  const u16* ldt; const u16* dsk; const u16* w1; const u16* w3; const u16* w2; float* out; char* ws;
};

__device__ __forceinline__ float bf2f(u16 b) { return __uint_as_float(((unsigned)b) << 16); }
__device__ __forceinline__ unsigned pk_bf16(float lo, float hi) { f32x2 v = {lo, hi}; bf16x2_t r = __builtin_convertvector(v, bf16x2_t); return __builtin_bit_cast(unsigned, r); }
__device__ __forceinline__ float bflo(unsigned p) { return __uint_as_float(p << 16); }
__device__ __forceinline__ float bfhi(unsigned p) { return __uint_as_float(p & 0xffff0000u); }
__device__ __forceinline__ float rbf(float f) { return bflo(pk_bf16(f, 0.f)); }
__device__ __forceinline__ float dot2(unsigned a, unsigned b, float c) { return __builtin_amdgcn_fdot2_f32_bf16(__builtin_bit_cast(bf16x2_t, a), __builtin_bit_cast(bf16x2_t, b), c, false); }
__device__ __forceinline__ f32x2 silu2(f32x2 v) { const f32x2 t = v * -1.44269504089f; f32x2 e; e.x = __builtin_amdgcn_exp2f(t.x); e.y = __builtin_amdgcn_exp2f(t.y);
  const f32x2 d = e + 1.0f; f32x2 r; r.x = __builtin_amdgcn_rcpf(d.x); r.y = __builtin_amdgcn_rcpf(d.y); return v * r; }

#define SCHED() __builtin_amdgcn_sched_barrier(0)
#define BAR() asm volatile("s_barrier" ::: "memory")
#define BAR_L() do { SCHED(); asm volatile("s_waitcnt lgkmcnt(0)\n\ts_barrier" ::: "memory"); SCHED(); } while (0)
template <int N> __device__ __forceinline__ void wait_v() { asm volatile("s_waitcnt vmcnt(%0)" :: "i"(N) : "memory"); }
#define DSPER 1
template <int ND, int NM> __device__ __forceinline__ void ilv() {
#pragma unroll
  for (int i = 0; i < (ND + DSPER - 1) / DSPER; ++i) { __builtin_amdgcn_sched_group_barrier(0x8, 1, 0); __builtin_amdgcn_sched_group_barrier(0x100, DSPER, 0); }
  __builtin_amdgcn_sched_group_barrier(0x8, ND + NM - (ND + DSPER - 1) / DSPER, 0); }
#define MFMA16(a, b, c) __builtin_amdgcn_mfma_f32_16x16x32_bf16(a, b, c, 0, 0, 0)
__device__ __forceinline__ void glds16(const void* g, LAS char* l) { __builtin_amdgcn_global_load_lds((const unsigned*)g, (LAS unsigned*)l, 16, 0, 0); }
__device__ __forceinline__ void glds16_dev(const void* g, LAS char* l) { __builtin_amdgcn_global_load_lds((const unsigned*)g, (LAS unsigned*)l, 16, 0, 16); }
__device__ __forceinline__ bf16x8 ld_row(const LAS char* p) { return *(const LAS bf16x8*)p; }
typedef short v4i16_t __attribute__((ext_vector_type(4)));
__device__ __forceinline__ s16x4 ld_tr4(const LAS char* p) { return __builtin_amdgcn_ds_read_tr16_b64_v4i16((LAS v4i16_t*)p); }
__device__ __forceinline__ bf16x8 ld_tr(const LAS char* p, int t1off) { const s16x4 lo = ld_tr4(p), hi = ld_tr4(p + t1off); return __builtin_shufflevector(lo, hi, 0, 1, 2, 3, 4, 5, 6, 7); }
#define K1AUX 16
__device__ __forceinline__ void st16_wt(__amdgpu_buffer_rsrc_t r, unsigned off, u32x4 v) { __builtin_amdgcn_raw_buffer_store_b128(v, r, off, 0, 16); }
__device__ __forceinline__ void st16_k1(__amdgpu_buffer_rsrc_t r, unsigned off, u32x4 v) { __builtin_amdgcn_raw_buffer_store_b128(v, r, off, 0, K1AUX); }
__device__ __forceinline__ void st8_wt(__amdgpu_buffer_rsrc_t r, unsigned off, u32x2 v) { __builtin_amdgcn_raw_buffer_store_b64(v, r, off, 0, 16); }
__device__ __forceinline__ void st4_wt(__amdgpu_buffer_rsrc_t r, unsigned off, unsigned v) { __builtin_amdgcn_raw_buffer_store_b32(v, r, off, 0, 16); }
__device__ __forceinline__ __amdgpu_buffer_rsrc_t mk_rsrc(const void* p, unsigned bytes) { return __builtin_amdgcn_make_buffer_rsrc((void*)p, 0, bytes, 0x00020000); }

#define ST_DECL()
#define ST()
#define STL()
#define ST_RT(slot)
#define ST_OUT(kern, wid_)

static constexpr int XSLOT = 32768, K1_WOFF = 3 * XSLOT, WSLOT = 10256, W3OFF = 5136, BSLOT = 16384, K1_RS = K1_WOFF + 6 * WSLOT  , EP_CS = K1_RS + 2048  , K1_PF = EP_CS + 640  , K1_LDS = K1_PF + 256;
static constexpr int SSM_TAB = K1_WOFF + 3 * BSLOT;
static constexpr int EP_W = 4096  , EP_G = 8192  ;

__device__ __forceinline__ unsigned long long mix64(unsigned long long z) { z += 0x9E3779B97F4A7C15ull; z = (z ^ (z >> 30)) * 0xBF58476D1CE4E5B9ull; z = (z ^ (z >> 27)) * 0x94D049BB133111EBull; return z ^ (z >> 31); }
__device__ __forceinline__ unsigned long long ld_dev(const void* p) { return __hip_atomic_load((gu64*)p, __ATOMIC_RELAXED, __HIP_MEMORY_SCOPE_AGENT); }
__device__ __forceinline__ void st_dev(void* p, unsigned long long v) { __hip_atomic_store((gu64*)p, v, __ATOMIC_RELAXED, __HIP_MEMORY_SCOPE_AGENT); }
__device__ __forceinline__ void touch(const void* g, unsigned lds_pad) { unsigned keep; asm volatile("s_mov_b32 %0, m0\n\ts_mov_b32 m0, %2\n\ts_nop 0\n\tglobal_load_lds_dword %1, off\n\ts_mov_b32 m0, %0" : "=&s"(keep) : "v"(g), "s"(lds_pad) : "memory"); }
__device__ __forceinline__ int ssm_token(int rho) { const int i = rho & 15, bb = (rho >> 4) & 3; return (rho & ~63) + 16 * (i >> 2) + 8 * (i & 1) + 2 * bb + ((i >> 1) & 1); }

__device__ __forceinline__ unsigned long long ld_sc(const void* p) { unsigned long long v; asm volatile("s_load_dwordx2 %0, %1, 0x0\n\ts_waitcnt lgkmcnt(0)" : "=s"(v) : "s"(p) : "memory"); return v; }
template <int C> __device__ __forceinline__ float dpp_add(float x) { return x + __int_as_float(__builtin_amdgcn_update_dpp(0, __float_as_int(x), C, 0xf, 0xf, true)); }
__device__ __forceinline__ float wave_sum(float x) {
  x = dpp_add<0xB1>(x); x = dpp_add<0x4E>(x); x = dpp_add<0x141>(x); x = dpp_add<0x140>(x);
  const int xi = __float_as_int(x);
  return (__int_as_float(__builtin_amdgcn_readlane(xi, 0)) + __int_as_float(__builtin_amdgcn_readlane(xi, 16))) + (__int_as_float(__builtin_amdgcn_readlane(xi, 32)) + __int_as_float(__builtin_amdgcn_readlane(xi, 48))); }

template <bool SSM>
__device__ __forceinline__ void k1_offsets(int lw, int lane, int cg, unsigned (&xo)[4], unsigned (&wo)[3]) {
#pragma unroll
  for (int j = 0; j < 4; ++j) { const int rho = (4 * lw + j) * 8 + (lane >> 3); xo[j] = ssm_token(rho) * 1280 + (((lane & 7) ^ (rho & 7)) << 4); }
  if (SSM) {
#pragma unroll
    for (int j = 0; j < 2; ++j) { const int rho = (2 * lw + j) * 8 + (lane >> 3); wo[j] = rho * 1280 + (((lane & 7) ^ (rho & 7)) << 4); }
    wo[2] = 0;
  } else { const int cmax = (F - cg * 80) - 8;
#pragma unroll
    for (int j = 0; j < 3; ++j) { const int r = ((lw & 1) * 3 + j) * 4 + (lane >> 4); const int ut = (r * 13) >> 6, n = r - 5 * ut;
      const int k = 16 * (ut >> 1) + 8 * ((lane >> 3) & 1) + 4 * (ut & 1) + ((lane >> 1) & 3); int col = 16 * n + 8 * (lane & 1); col = col < cmax ? col : cmax; wo[j] = (k & 31) * (F * 2) + col * 2; }
  }
}
__device__ __forceinline__ void glds16_asm(const void* g, unsigned lds_dst) { unsigned keep; asm volatile("s_mov_b32 %0, m0\n\ts_mov_b32 m0, %2\n\ts_nop 0\n\tglobal_load_lds_dwordx4 %1, off\n\ts_mov_b32 m0, %0" : "=&s"(keep) : "v"(g), "s"(lds_dst) : "memory"); }

template <bool SSM>
__device__ __forceinline__ void k1_loader(const Args& a, LAS char* lds, int lw, int lane, int rb, int cg, unsigned long long epoch) {
  ST_DECL(); ST_RT(62); ST();
  unsigned xo[4], wo[3];
  k1_offsets<SSM>(lw, lane, cg, xo, wo);
  const char* xb = (const char*)a.x + (size_t)rb * 256 * 1280;
  const char* wb = (const char*)(lw < 2 ? a.w1 : a.w3) + cg * 160;
  const char* bre = (const char*)a.bre + (size_t)cg * 64 * 1280; const char* bim = (const char*)a.bim + (size_t)cg * 64 * 1280;
  const int wdst = K1_WOFF + (lw >> 1) * W3OFF + (lw & 1) * 3072;
  auto issue_w = [&](int u) { if (!SSM) { LAS char* st = lds + wdst + (u % 6) * WSLOT; const char* src = wb + (size_t)u * (32 * F * 2);
      glds16(src + wo[0], st); glds16(src + wo[1], st + 1024); if (!(lw & 1)) glds16(src + wo[2], st + 2048); } };
  auto issue_x = [&](int j) { const int sl = (j + 5) >> 1, part = (j + 1) & 1; LAS char* st = lds + (sl % 3) * XSLOT + part * 16384 + lw * 4096; const char* src = xb + part * (128 * 1280) + sl * 128;
#pragma unroll
    for (int q = 0; q < 4; ++q) glds16(src + xo[q], st + q * 1024);
    if (SSM) { LAS char* sb = lds + K1_WOFF + (sl % 3) * BSLOT + part * 8192 + lw * 2048; const char* sr = (part ? bim : bre) + sl * 128; glds16(sr + wo[0], sb); glds16(sr + wo[1], sb + 1024); } };
#define WAITG(NWI, NXI, EX) do { if (SSM) wait_v<(NXI) * 6 + (EX)>(); else if (lw & 1) wait_v<(NWI) * 2 + (NXI) * 4 + (EX)>(); else wait_v<(NWI) * 3 + (NXI) * 4 + (EX)>(); } while (0)
  ST();
  issue_w(0); issue_x(-5); issue_x(-4); issue_w(1);
  issue_x(-3); issue_w(2);
  const bf16x8 ones = {0x3f80, 0x3f80, 0x3f80, 0x3f80, 0x3f80, 0x3f80, 0x3f80, 0x3f80};
  f32x4 cs[3] = {{0.f, 0.f, 0.f, 0.f}, {0.f, 0.f, 0.f, 0.f}, {0.f, 0.f, 0.f, 0.f}};
  const unsigned lds0 = (unsigned)(uintptr_t)lds;
  const unsigned cbase = SSM ? lds0 + K1_WOFF + (32 * lw + (lane & 15)) * 128 + (((lane >> 4) ^ (lane & 7)) << 4)
                             : lds0 + K1_WOFF + (lw >> 1) * W3OFF + (lane >> 5) * 2560 + (lane & 31) * 8 + (lw & 1) * 768;
  u32x2 c0, c1, c2, c3, c4, c5; u32x4 d0, d1;
  auto cs_read = [&](int u) {
    if (SSM) { const unsigned p = (cbase + ((u >> 1) % 3) * BSLOT) ^ ((u & 1) * 64);
      asm volatile("ds_read_b128 %0, %2\n\tds_read_b128 %1, %2 offset:2048" : "=&v"(d0), "=&v"(d1) : "v"(p) : "memory");
    } else { const unsigned p = cbase + (u % 6) * WSLOT;
      asm volatile("ds_read_b64_tr_b16 %0, %6\n\tds_read_b64_tr_b16 %1, %6 offset:1280\n\tds_read_b64_tr_b16 %2, %6 offset:256\n\tds_read_b64_tr_b16 %3, %6 offset:1536\n\tds_read_b64_tr_b16 %4, %6 offset:512\n\tds_read_b64_tr_b16 %5, %6 offset:1792"
                   : "=&v"(c0), "=&v"(c1), "=&v"(c2), "=&v"(c3), "=&v"(c4), "=&v"(c5) : "v"(p) : "memory"); } };
  auto cs_acc = [&]() {
    __builtin_amdgcn_s_setprio(3);
    if (SSM) { asm volatile("s_waitcnt lgkmcnt(0)" : "+v"(d0), "+v"(d1) :: "memory");
      cs[0] = MFMA16(ones, __builtin_bit_cast(bf16x8, d0), cs[0]); cs[1] = MFMA16(ones, __builtin_bit_cast(bf16x8, d1), cs[1]);
    } else { asm volatile("s_waitcnt lgkmcnt(0)" : "+v"(c0), "+v"(c1), "+v"(c2), "+v"(c3), "+v"(c4), "+v"(c5) :: "memory");
      cs[0] = MFMA16(ones, __builtin_bit_cast(bf16x8, ((u32x4){c0.x, c0.y, c1.x, c1.y})), cs[0]);
      cs[1] = MFMA16(ones, __builtin_bit_cast(bf16x8, ((u32x4){c2.x, c2.y, c3.x, c3.y})), cs[1]);
      if (!(lw & 1)) cs[2] = MFMA16(ones, __builtin_bit_cast(bf16x8, ((u32x4){c4.x, c4.y, c5.x, c5.y})), cs[2]); }
    SCHED();
    __builtin_amdgcn_s_setprio(0);
  };
#define BAR_LD() asm volatile("s_waitcnt lgkmcnt(0)\n\ts_barrier" ::: "memory")
  ST();
  WAITG(1, 1, 0);
  ST();
  BAR();
  cs_read(0);
  BAR_LD();
  ST();
  cs_acc(); cs_read(1);
  issue_x(-2); issue_w(3); issue_x(-1);
  WAITG(1, 1, 0);
  ST();
  BAR_LD();
  cs_acc(); cs_read(2);
  issue_w(4); issue_x(0); issue_w(5);
  WAITG(2, 2, 0);
  ST();
  BAR_LD();
  cs_acc(); cs_read(3);
  issue_w(6); issue_x(1); issue_w(7);
  WAITG(3, 1, 0);
  ST();
  BAR_LD();
  cs_acc(); cs_read(4);
  issue_x(2); issue_w(8); issue_x(3); issue_w(9);
  WAITG(4, 3, 0);
  ST();
  BAR_LD();
  ST();
#pragma unroll 1
  for (int j = 4; j < 14; ++j) {
    cs_acc(); cs_read(j + 1);
    issue_x(j); issue_w(j + 6);
    WAITG(2, 2, 0);
    BAR_LD();
  }
  cs_acc(); cs_read(15); issue_x(14); WAITG(1, 2, 0); BAR_LD();
  cs_acc(); cs_read(16); WAITG(0, 1, 0);
  const int rho = 64 * lw + lane; const char* gp = a.ws + WS_RS + (size_t)(rb * 256 + ssm_token(rho)) * 16;
  unsigned long long g0 = ld_dev(gp), g1 = ld_dev(gp + 8);
  BAR_LD();
  cs_acc(); cs_read(17); wait_v<2>(); BAR_LD();
  cs_acc(); cs_read(18); BAR_LD();
  cs_acc(); cs_read(19);
  { const unsigned long long tg = epoch & 0xffffffffffffull; unsigned spins = 0;
    while (((g0 >> 16) != tg || (g1 >> 16) != tg) && ++spins < SPIN_MAX) { __builtin_amdgcn_s_sleep(1); g0 = ld_dev(gp); g1 = ld_dev(gp + 8); }
    ((LAS f32x2*)(lds + K1_RS))[rho] = (f32x2){__uint_as_float((unsigned)g0 << 16), 1.0f / __uint_as_float((unsigned)g1 << 16)}; }
  cs_acc();
  if (SSM) { if (lane < 16) { ((LAS float*)(lds + EP_CS))[32 * lw + lane] = cs[0][0]; ((LAS float*)(lds + EP_CS))[32 * lw + 16 + lane] = cs[1][0]; } }
  else if (lane < 16) { LAS float* o = (LAS float*)(lds + EP_CS) + (lw >> 1) * 80 + (lw & 1) * 48 + lane; o[0] = cs[0][0]; o[16] = cs[1][0]; if (!(lw & 1)) o[32] = cs[2][0]; }
  BAR_LD();
  ST();
  if (!SSM) {
    const int tid = lw * 64 + lane; const auto wr = mk_rsrc(a.ws + WS_W2 + (size_t)rb * 276480, 276480);
    u32x4 v[4]; int cc_[4];
#pragma unroll
    for (int q = 0; q < 4; ++q) { int c = cg * 786 + q * 256 + tid; const bool ok = (q * 256 + tid < 786) && c < 17280; c = ok ? c : cg * 786; cc_[q] = ok ? c * 16 : 0x7ffffff0;
      const int s = c / 640, cc = c % 640, e = cc & 1, qq = (cc >> 1) & 3, g1 = (cc >> 3) & 1, r = cc >> 4, n = r % 5, ut = r / 5, t = ut & 1, u = ut >> 1;
      const int f = 64 * s + 16 * u + 8 * g1 + 4 * t + qq, d = 80 * rb + 16 * n + 8 * e;
      v[q] = *(const u32x4*)(a.w2 + (size_t)f * D + d); }
#pragma unroll
    for (int q = 0; q < 4; ++q) st16_k1(wr, cc_[q], v[q]);
  }
  if (SSM) { BAR(); if (rb > 0) BAR(); }
  if (SSM) {
    const int i = ((cg * 8 + rb) * 4 + lw) * 64 + lane;
    if (i < 12800) { const char* p = (const char*)(i < 6400 ? a.cre : a.cim) + (size_t)(i < 6400 ? i : i - 6400) * 128; unsigned t; asm volatile("global_load_dword %0, %1, off\n\ts_waitcnt vmcnt(0)" : "=&v"(t) : "v"(p) : "memory"); }
  }
  ST_RT(63); ST_OUT(1, 8 + lw);
}

template <bool SSM, int WN>
__device__ __forceinline__ void k1_compute(const Args& a, LAS char* lds, int wid, int lane, int rb, int cg, int ci, unsigned long long epoch) {
  const int wm = wid & 3, i16 = lane & 15, g = lane >> 4; constexpr int wn = WN;
  constexpr int NC = SSM ? 4 : 5;
  ST_DECL(); ST_RT(62); ST();
  const unsigned lds0 = (unsigned)(uintptr_t)lds;
  {
    const int row = rb * 256 + 8 * (SSM ? 22 + cg : cg) + wid; const unsigned* xr = (const unsigned*)(a.x + (size_t)row * D);
    unsigned v[5]; u16 t_dt = 0, t_re = 0, t_im = 0;
#pragma unroll
    for (int q = 0; q < 5; ++q) v[q] = xr[lane + 64 * q];
    if (SSM) { const int p = cg * 64 + lane; t_dt = a.ldt[p]; t_re = a.lre[p]; t_im = a.lim[p]; }
    const float x0 = __uint_as_float(__builtin_amdgcn_readfirstlane(v[0]) << 16);
    float s1 = 0.f, s2 = 0.f;
#pragma unroll
    for (int q = 0; q < 5; ++q) { const float d0 = bflo(v[q]) - x0, d1 = bfhi(v[q]) - x0; s1 += d0 + d1; s2 += d0 * d0 + d1 * d1; }
    const float dm = wave_sum(s1) * (1.0f / D), var = fmaxf(wave_sum(s2) * (1.0f / D) - dm * dm, 0.f);
    const unsigned mb = pk_bf16(x0 + dm, 0.f) & 0xffff, sb = pk_bf16(__builtin_sqrtf(rbf(rbf(var) + 1e-5f)), 0.f) & 0xffff;
    if (lane < 2) st_dev(a.ws + WS_RS + (size_t)row * 16 + lane * 8, (epoch << 16) | (lane ? sb : mb));
    if (SSM && wid == 0) {
      const float dt = rbf(__expf(bf2f(t_dt))), ar = bf2f(t_re), ai = bf2f(t_im);
      const float e = __expf(dt * ar), th = dt * ai;
      const float kq = __builtin_rintf(th * 0.15915494309f), rv = __builtin_fmaf(th, 0.15915494309f, -kq) + th * 6.4206383e-9f;
      const float sn = __builtin_amdgcn_sinf(rv), cs = __builtin_amdgcn_cosf(rv);
      float pr = e * cs, pi = e * sn;
      const float nr = pr - 1.0f, ni = pi, den = 2.0f / (ar * ar + ai * ai);
      LAS f32x2* tab = (LAS f32x2*)(lds + SSM_TAB) + lane;
      tab[0] = (f32x2){pr, pi}; tab[64] = (f32x2){(nr * ar + ni * ai) * den, (ni * ar - nr * ai) * den};
#define CSQ() do { const float t_ = pr * pr - pi * pi; pi = 2.0f * pr * pi; pr = t_; } while (0)
      CSQ(); CSQ(); CSQ(); tab[128] = (f32x2){pr, pi};
      CSQ(); tab[192] = (f32x2){pr, pi}; const float ar16 = pr, ai16 = pi;
      CSQ(); tab[256] = (f32x2){pr, pi};
      tab[320] = (f32x2){pr * ar16 - pi * ai16, pr * ai16 + pi * ar16};
      CSQ(); tab[384] = (f32x2){pr, pi};
      CSQ(); CSQ(); tab[448] = (f32x2){pr, pi};
    }
  }
  f32x4 acc[4][NC];
#pragma unroll
  for (int b = 0; b < 4; ++b)
#pragma unroll
    for (int c = 0; c < NC; ++c) acc[b][c] = (f32x4){0.f, 0.f, 0.f, 0.f};
  const int xo0 = (64 * wm + i16) * 128 + ((g ^ (lane & 7)) << 4);
  int wo0, woM = 0;
  if (SSM) wo0 = K1_WOFF + (32 * wn + i16) * 128 + ((g ^ (lane & 7)) << 4);
  else { wo0 = K1_WOFF + (lane >> 5) * 2560 + (lane & 31) * 8 + wn * 768; const int p = lane & 3;
    woM = K1_WOFF + (lane >> 5) * 2560 + 512 + ((lane & 31) >> 2) * 32 + (p < 2 ? (2 * wn + p) * 8 : W3OFF + (2 * wn + p - 2) * 8); }
  bf16x8 X0[4], W0[NC], X1[4], W1[NC];
  int xs = 0, ws = 0;
  auto rd = [&](bf16x8 (&X)[4], bf16x8 (&W)[NC], int h) {
    const LAS char* xp = lds + xs + (xo0 ^ (h * 64));
    if (SSM) { const LAS char* wp = lds + (xs >> 1) + (wo0 ^ (h * 64));
      W[0] = ld_row(wp); W[1] = ld_row(wp + 2048); W[2] = ld_row(wp + 8192); W[3] = ld_row(wp + 8192 + 2048);
#pragma unroll
      for (int b = 0; b < 4; ++b) X[b] = ld_row(xp + b * 2048);
    } else { const LAS char* wp = lds + ws + wo0; const LAS char* mp = lds + ws + woM;
#pragma unroll
      for (int b = 0; b < 4; ++b) X[b] = ld_row(xp + b * 2048);
      W[0] = ld_tr(wp, 1280); W[1] = ld_tr(wp + 256, 1280); W[2] = ld_tr(wp + W3OFF, 1280); W[3] = ld_tr(wp + W3OFF + 256, 1280); W[NC - 1] = ld_tr(mp, 1280); }
    ws = ws + WSLOT == 6 * WSLOT ? 0 : ws + WSLOT;
    if (h) xs = xs + XSLOT == 3 * XSLOT ? 0 : xs + XSLOT; };
  auto mm = [&](const bf16x8 (&X)[4], const bf16x8 (&W)[NC]) {
#pragma unroll
    for (int c = 0; c < NC; ++c)
#pragma unroll
      for (int b = 0; b < 4; ++b) acc[b][c] = MFMA16(X[b], W[c], acc[b][c]); };
  constexpr int ND = SSM ? 8 : 14, NM = SSM ? 8 : 6;
  ST();
  const char* pfp = SSM ? (const char*)(((lane >> 4) & 1) ? a.bim : a.bre) + (size_t)(cg * 64 + 16 * (rb & 3) + (lane & 15)) * 1280 + (lane >> 5) * 128
                        : (const char*)(((lane >> 1) & 1) ? a.w3 : a.w1) + (size_t)(16 * (rb & 3) + (lane >> 2)) * (F * 2) + cg * 160 + (lane & 1) * (cg == 21 ? 92 : 156);
  constexpr int PFS = SSM ? 128 : 64 * F * 2;
  BAR();
  ST();
  rd(X0, W0, 0);
  if (SSM) { if (wid == 7) touch(pfp + 3 * PFS, lds0 + K1_PF); } else { if (wid >= 6) touch(pfp + (wid - 3) * PFS, lds0 + K1_PF); }
  SCHED();
  SCHED();
#pragma unroll 1
  for (int j = 0; j < 20; j += 2) {
    BAR_L(); STL(); rd(X1, W1, 1); mm(X0, W0); ilv<ND, NM>();
    SCHED();
    if (SSM) { if (j <= 4 && (j & 2) == 0 && wid == (j >> 1)) touch(pfp + (5 + (j >> 1)) * PFS, lds0 + K1_PF); }
    else if (j < 10 && wid == (j >> 1)) touch(pfp + (5 + (j >> 1)) * PFS, lds0 + K1_PF);
    BAR_L(); STL(); rd(X0, W0, 0); mm(X1, W1); ilv<ND, NM>();
  }
  SCHED();
  ST();
  const int tid = wid * 64 + lane;
  const LAS f32x2* rst = (const LAS f32x2*)(lds + K1_RS);
  if constexpr (!SSM) {
    const auto hr = mk_rsrc(a.ws + WS_HM + (size_t)(2 * rb + (wm >> 1)) * (27 * 16384), 27 * 16384);
    const LAS float* csp = (const LAS float*)(lds + EP_CS);
    const bool lo = i16 < 8;
    float ng[2], nu[2];
#pragma unroll
    for (int j = 0; j < 2; ++j) { ng[j] = -csp[16 * (3 * wn + j) + i16]; nu[j] = -csp[80 + 16 * (3 * wn + j) + i16]; }
    const float nm = -csp[(lo ? 32 : 104) + 8 * wn + i16];
    u32x4 oA[2], oB[2], oM;
#pragma unroll
    for (int b = 0; b < 4; ++b) {
      const f32x4 p0 = *(const LAS f32x4*)(rst + 64 * wm + 16 * b + 4 * g), p1 = *(const LAS f32x4*)(rst + 64 * wm + 16 * b + 4 * g + 2);
      const f32x4 m = {p0[0], p0[2], p1[0], p1[2]}, r = {p0[1], p0[3], p1[1], p1[3]}, rn = r * -1.44269504089f, r2 = r * r;
      auto act = [&](float tg, float tu, float rn_, float r2_) { return (tg * tu) * r2_ * __builtin_amdgcn_rcpf(1.0f + __builtin_amdgcn_exp2f(tg * rn_)); };
#pragma unroll
      for (int j = 0; j < 2; ++j) { const f32x4 tg = acc[b][j] + m * ng[j], tu = acc[b][2 + j] + m * nu[j];
        oA[j][b] = pk_bf16(act(tg[0], tu[0], rn[0], r2[0]), act(tg[2], tu[2], rn[2], r2[2])); oB[j][b] = pk_bf16(act(tg[1], tu[1], rn[1], r2[1]), act(tg[3], tu[3], rn[3], r2[3])); }
      { const f32x4 v = acc[b][4] + m * nm;
        const float s0 = lo ? v[1] : v[0], s1 = lo ? v[3] : v[2], a0 = lo ? v[0] : v[1], a1 = lo ? v[2] : v[3];
        const float q0 = __int_as_float(__builtin_amdgcn_update_dpp(0, __float_as_int(s0), 0x128, 0xf, 0xf, false)), q1 = __int_as_float(__builtin_amdgcn_update_dpp(0, __float_as_int(s1), 0x128, 0xf, 0xf, false));
        const float g0 = lo ? a0 : q0, g1 = lo ? a1 : q1, u0 = lo ? q0 : a0, u1 = lo ? q1 : a1;
        oM[b] = pk_bf16(act(g0, u0, lo ? rn[0] : rn[1], lo ? r2[0] : r2[1]), act(g1, u1, lo ? rn[2] : rn[3], lo ? r2[2] : r2[3])); }
    }
    const unsigned nn = 4 * (wm & 1) + g, tq = ((i16 >> 2) & 1) * 2048 + nn * 256 + (i16 & 3) * 16;
#pragma unroll
    for (int j = 0; j < 2; ++j) { const int f0 = 80 * cg + 16 * (3 * wn + j);
      if (f0 < F) { const unsigned o = (f0 >> 6) * 16384 + ((f0 >> 4) & 3) * 4096 + (i16 >> 3) * 128 + tq; st16_k1(hr, o, oA[j]); st16_k1(hr, o + 64, oB[j]); } }
    { const int f0 = 80 * cg + 32 + 8 * wn; st16_k1(hr, (f0 >> 6) * 16384 + ((f0 >> 4) & 3) * 4096 + ((f0 >> 3) & 1) * 128 + (i16 >> 3) * 64 + tq, oM); }
  } else {
    const LAS f32x2* tab = (const LAS f32x2*)(lds + SSM_TAB);
    f32x2 lam[2], l8[2];
#pragma unroll
    for (int b = 0; b < 4; ++b) { const f32x4 p0 = *(const LAS f32x4*)(rst + 64 * wm + 16 * b + 4 * g), p1 = *(const LAS f32x4*)(rst + 64 * wm + 16 * b + 4 * g + 2);
      const f32x4 m = {p0[0], p0[2], p1[0], p1[2]}, r = {p0[1], p0[3], p1[1], p1[3]};
#pragma unroll
      for (int n = 0; n < 2; ++n) { const int ch = 32 * wn + 16 * n + i16; const float csr = -((const LAS float*)(lds + EP_CS))[ch], csi = -((const LAS float*)(lds + EP_CS))[64 + ch];
        acc[b][n] = (acc[b][n] + m * csr) * r; acc[b][2 + n] = (acc[b][2 + n] + m * csi) * r; } }
    f32x2 ea[2], ex[2], inc[2];
#pragma unroll
    for (int n = 0; n < 2; ++n) { const int ch = 32 * wn + 16 * n + i16;
      lam[n] = tab[ch]; l8[n] = tab[128 + ch]; const f32x2 l16 = tab[192 + ch], l32 = tab[256 + ch];
      const f32x2 Lr = {lam[n].x, lam[n].x}, Li = {lam[n].y, lam[n].y};
      f32x2 hr = {acc[0][n][0], acc[0][n][1]}, hi = {acc[0][2 + n][0], acc[0][2 + n][1]};
#pragma unroll
      for (int s = 1; s < 8; ++s) { const int b = s >> 1, q = s & 1; const f32x2 ur = {acc[b][n][2 * q], acc[b][n][2 * q + 1]}, ui = {acc[b][2 + n][2 * q], acc[b][2 + n][2 * q + 1]};
        const f32x2 nr = Lr * hr - Li * hi + ur, ni = Lr * hi + Li * hr + ui; hr = nr; hi = ni; }
      ea[n] = (f32x2){hr.x, hi.x};
      float er = l8[n].x * hr.x - l8[n].y * hi.x + hr.y, ei = l8[n].x * hi.x + l8[n].y * hr.x + hi.y;
      { float pr = __shfl_up(er, 16), pi = __shfl_up(ei, 16); if (g < 1) { pr = 0.f; pi = 0.f; }
        er += l16.x * pr - l16.y * pi; ei += l16.x * pi + l16.y * pr; }
      { float pr = __shfl_up(er, 32), pi = __shfl_up(ei, 32); if (g < 2) { pr = 0.f; pi = 0.f; }
        er += l32.x * pr - l32.y * pi; ei += l32.x * pi + l32.y * pr; }
      inc[n] = (f32x2){er, ei};
      { float pr = __shfl_up(er, 16), pi = __shfl_up(ei, 16); if (g < 1) { pr = 0.f; pi = 0.f; } ex[n] = (f32x2){pr, pi}; }
      if (g == 3) ((LAS f32x2*)(lds + EP_W))[wm * 64 + ch] = inc[n];
    }
    ST();
    BAR_L();
    f32x2 cw[2], pw[2];
#pragma unroll
    for (int n = 0; n < 2; ++n) { const int ch = 32 * wn + 16 * n + i16; const f32x2 l64 = tab[384 + ch];
      const f32x2 e0 = ((const LAS f32x2*)(lds + EP_W))[ch], e1 = ((const LAS f32x2*)(lds + EP_W))[64 + ch], e2 = ((const LAS f32x2*)(lds + EP_W))[128 + ch];
      float xr = 0.f, xi = 0.f, qr = 1.f, qi = 0.f;
      auto hstep = [&](f32x2 e) { const float t = l64.x * xr - l64.y * xi + e.x; xi = l64.x * xi + l64.y * xr + e.y; xr = t; const float u = l64.x * qr - l64.y * qi; qi = l64.x * qi + l64.y * qr; qr = u; };
      if (wm > 0) hstep(e0);
      if (wm > 1) hstep(e1);
      if (wm > 2) hstep(e2);
      if (wm == 3 && g == 3) { const float Ar = l64.x * xr - l64.y * xi + inc[n].x, Ai = l64.x * xi + l64.y * xr + inc[n].y;
        const unsigned long long v = ((unsigned long long)__float_as_uint(Ai) << 32) | __float_as_uint(Ar), tg = epoch << 22;
        char* gp = a.ws + WS_AG + (((size_t)cg * 8 + rb) * 192 + ch * 3) * 8;
        st_dev(gp, tg | (v & 0x3fffff)); st_dev(gp + 8, tg | ((v >> 22) & 0x3fffff)); st_dev(gp + 16, tg | (v >> 44)); }
      const f32x2 lg = g == 0 ? (f32x2){1.f, 0.f} : tab[128 + 64 * g + ch];
      cw[n] = (f32x2){ex[n].x + lg.x * xr - lg.y * xi, ex[n].y + lg.x * xi + lg.y * xr};
      pw[n] = (f32x2){lg.x * qr - lg.y * qi, lg.x * qi + lg.y * qr}; }
    const bool gat = (tid >> 6) < rb; const char* ggp = a.ws + WS_AG + (((size_t)cg * 8 + (tid >> 6)) * 192 + (tid & 63) * 3) * 8;
    unsigned long long gv0 = 0, gv1 = 0, gv2 = 0;
    if (gat) { gv0 = ld_dev(ggp); gv1 = ld_dev(ggp + 8); gv2 = ld_dev(ggp + 16); }
    ST();
#pragma unroll
    for (int n = 0; n < 2; ++n) { const f32x2 cf = tab[64 + 32 * wn + 16 * n + i16];
      const f32x2 Lr = {lam[n].x, lam[n].x}, Li = {lam[n].y, lam[n].y}, Cr = {cf.x, cf.x}, Ci = {cf.y, cf.y}, nCr = -Cr, nCi = -Ci;
      f32x2 hr = {cw[n].x, l8[n].x * cw[n].x - l8[n].y * cw[n].y + ea[n].x}, hi = {cw[n].y, l8[n].x * cw[n].y + l8[n].y * cw[n].x + ea[n].y};
#pragma unroll
      for (int s = 0; s < 8; ++s) { const int b = s >> 1, q = s & 1; const f32x2 ur = {acc[b][n][2 * q], acc[b][n][2 * q + 1]}, ui = {acc[b][2 + n][2 * q], acc[b][2 + n][2 * q + 1]};
        const f32x2 nr = Lr * hr - Li * hi + ur, ni = Lr * hi + Li * hr + ui; hr = nr; hi = ni;
        const f32x2 vr = Cr * hr + nCi * hi, vi = nCr * hi + nCi * hr;
        acc[b][n][2 * q] = vr.x; acc[b][n][2 * q + 1] = vr.y; acc[b][2 + n][2 * q] = vi.x; acc[b][2 + n][2 * q + 1] = vi.y; } }
    ST();
    if (rb > 0) {
      if (gat) { const unsigned long long tg = epoch & 0x3ffffffffffull; unsigned spins = 0;
#pragma unroll 1
        while (((gv0 >> 22) != tg || (gv1 >> 22) != tg || (gv2 >> 22) != tg) && ++spins < SPIN_MAX) { __builtin_amdgcn_s_sleep(1); gv0 = ld_dev(ggp); gv1 = ld_dev(ggp + 8); gv2 = ld_dev(ggp + 16); }
        const unsigned p0 = (unsigned)gv0 & 0x3fffff, p1 = (unsigned)gv1 & 0x3fffff, p2 = (unsigned)gv2 & 0x3fffff;
        ((LAS u32x2*)(lds + EP_G))[tid] = (u32x2){p0 | (p1 << 22), (p1 >> 10) | (p2 << 12)}; }
      ST();
      BAR_L();
      ST();
#pragma unroll
      for (int n = 0; n < 2; ++n) { const int ch = 32 * wn + 16 * n + i16; const f32x2 z = tab[448 + ch], cf = tab[64 + ch];
        float Gr = 0.f, Gi = 0.f;
        f32x2 ag[7];
#pragma unroll
        for (int rp = 0; rp < 7; ++rp) ag[rp] = ((const LAS f32x2*)(lds + EP_G))[rp * 64 + ch];
#pragma unroll
        for (int rp = 0; rp < 7; ++rp) if (rp < rb) { const float t = z.x * Gr - z.y * Gi + ag[rp].x; Gi = z.x * Gi + z.y * Gr + ag[rp].y; Gr = t; }
        const float Zr = pw[n].x * Gr - pw[n].y * Gi, Zi = pw[n].x * Gi + pw[n].y * Gr;
        const float ar = cf.x * Zr - cf.y * Zi, ai = cf.x * Zi + cf.y * Zr;
        f32x2 wr = {ar, l8[n].x * ar - l8[n].y * ai}, wi = {-ai, -(l8[n].x * ai + l8[n].y * ar)};
        const f32x2 Lr = {lam[n].x, lam[n].x}, Li = {lam[n].y, lam[n].y};
#pragma unroll
        for (int s = 0; s < 8; ++s) { const int b = s >> 1, q = s & 1; const f32x2 nr = Lr * wr + Li * wi, ni = Lr * wi - Li * wr; wr = nr; wi = ni;
          acc[b][n][2 * q] += wr.x; acc[b][n][2 * q + 1] += wr.y; acc[b][2 + n][2 * q] += wi.x; acc[b][2 + n][2 * q + 1] += wi.y; } }
    }
    const auto tr = mk_rsrc(a.ws + WS_HT + (size_t)(2 * rb + (wm >> 1)) * (20 * 16384), 20 * 16384);
#pragma unroll
    for (int n = 0; n < 2; ++n) {
      u32x4 reA, reB, imA, imB;
#pragma unroll
      for (int b = 0; b < 4; ++b) { reA[b] = pk_bf16(acc[b][n][0], acc[b][n][2]); reB[b] = pk_bf16(acc[b][n][1], acc[b][n][3]); imA[b] = pk_bf16(acc[b][2 + n][0], acc[b][2 + n][2]); imB[b] = pk_bf16(acc[b][2 + n][1], acc[b][2 + n][3]); }
      const int u = 2 * wn + n, g1 = (i16 >> 3) & 1, t = (i16 >> 2) & 1, qq = i16 & 3, nn = 4 * (wm & 1) + g;
      const unsigned bo = (((u * 2 + t) * 8 + nn) * 256) + g1 * 128 + qq * 16 + cg * 16384;
      st16_k1(tr, bo, reA); st16_k1(tr, bo + 64, reB); st16_k1(tr, bo + 10 * 16384, imA); st16_k1(tr, bo + 10 * 16384 + 64, imB);
    }
  }
  ST();
  ST(); ST_RT(63); ST_OUT(1, wid);
}

static constexpr int K2_STAGE = 26624, K2_NB = 6, K2_LDS = K2_NB * K2_STAGE;

template <bool YW>
__device__ __forceinline__ void k2_loader(const Args& a, LAS char* lds, int lw, int lane, int rb, int dg, unsigned long long epoch) {
  constexpr int T = YW ? 20 : 27;
  ST_DECL(); ST_RT(62); ST();
  const char* tb = a.ws + (YW ? WS_HT + (size_t)rb * 20 * 16384 : WS_HM + (size_t)rb * 27 * 16384) + lane * 16;
  const unsigned co = YW ? (80 * dg + (lane >> 3)) * 1280 + (((lane & 7) ^ (lane >> 3)) << 4) : lane * 16; constexpr unsigned cstep = YW ? 8 * 1280 : 1024;
  auto dbase = [&](int s) { return YW ? (const char*)(s < 10 ? a.cre : a.cim) + (s % 10) * 128 : a.ws + WS_W2 + ((size_t)dg * 27 + s) * 10240; };
  const int d0 = lw < 2 ? 3 * lw : 6 + 2 * (lw - 2);
  auto issue_d = [&](int s) { LAS char* st = lds + (s % K2_NB) * K2_STAGE; const char* ds = dbase(s) + co + d0 * cstep;
    if (YW) { glds16(ds, st + 16384 + d0 * 1024); glds16(ds + cstep, st + 16384 + (d0 + 1) * 1024); if (lw < 2) glds16(ds + 2 * cstep, st + 16384 + (d0 + 2) * 1024); }
    else { glds16_dev(ds, st + 16384 + d0 * 1024); glds16_dev(ds + cstep, st + 16384 + (d0 + 1) * 1024); if (lw < 2) glds16_dev(ds + 2 * cstep, st + 16384 + (d0 + 2) * 1024); } };
  auto issue_t = [&](int s) { LAS char* st = lds + (s % K2_NB) * K2_STAGE; const char* ts = tb + (size_t)s * 16384 + lw * 4096;
#pragma unroll
    for (int j = 0; j < 4; ++j) glds16_dev(ts + j * 1024, st + lw * 4096 + j * 1024); };
  auto issue = [&](int s) { issue_d(s); issue_t(s); };
#define WAITK(k, ex) do { if (lw < 2) wait_v<(k) * 7 + (ex)>(); else wait_v<(k) * 6 + (ex)>(); } while (0)
  if (YW) { issue_d(0); issue_d(1); issue_d(2); issue_d(3); }
  {
    const int rbb = rb >> 1; const bool on = YW ? lane < 10 : lane < 44;
    const char* fp = a.ws + WS_DN + (size_t)(YW ? rbb * 32 + 22 + lane : (lane < 22 ? rbb * 32 + lane : dg * 32 + lane - 22)) * 64;
    unsigned spins = 0;
    while (__builtin_amdgcn_ballot_w64(on && ld_dev(on ? fp : a.ws + WS_DN) != epoch) != 0ull && ++spins < SPIN_MAX) __builtin_amdgcn_s_sleep(2);
    asm volatile("" ::: "memory"); }
  ST();
  if (YW) { issue_t(0); issue_t(1); wait_v<4>(); }
  else { issue(0); issue(1); WAITK(1, 0); }
  ST();
  BAR();
  if (YW) { issue_t(2); issue_t(3); wait_v<8>(); } else { issue(2); issue(3); WAITK(2, 0); }
  BAR();
  if (YW) { issue(4); if (lw < 2) wait_v<11>(); else wait_v<10>(); } else { issue(4); WAITK(2, 0); }
  BAR();
#pragma unroll 1
  for (int t = 1; t < T - 4; ++t) { issue(t + 4); WAITK(2, 0); BAR(); }
  if (!YW) { LAS char* st = lds + ((T - 5) % K2_NB) * K2_STAGE;
#pragma unroll
    for (int j = 0; j < 5; ++j) { const int c = (lw * 5 + j) * 64 + lane, row = (c * 6554) >> 16, ch = c - 10 * row; glds16((const char*)a.x + (size_t)(rb * 128 + row) * 1280 + dg * 160 + ch * 16, st + (lw * 5 + j) * 1024); }
    glds16_dev(a.ws + WS_RS + (size_t)(rb * 128 + (lw & 1) * 64 + lane) * 16, st + 20480 + (lw & 1) * 1024); }
  WAITK(1, YW ? 0 : 6); BAR();
  WAITK(0, YW ? 0 : 6); BAR();
  wait_v<0>(); BAR();
  if (blockIdx.x == 0 && lw == 3) {
    unsigned spins = 0; bool ok;
    do { ok = true;
#pragma unroll
      for (int q = 0; q < 4; ++q) ok &= ld_dev(a.ws + WS_DN + (size_t)(q * 64 + lane) * 64) == epoch;
    } while (__builtin_amdgcn_ballot_w64(!ok) != 0ull && ++spins < SPIN_MAX);
    if (lane == 0) st_dev(a.ws + WS_EP, epoch); }
  ST_RT(63); ST_OUT(2, 8 + lw);
}

template <bool YW>
__device__ __forceinline__ void k2_compute(const Args& a, LAS char* lds, int wid, int lane, int rb, int dg, int pair, unsigned long long epoch) {
  constexpr int T = YW ? 20 : 27;
  const int i16 = lane & 15, g = lane >> 4;
  ST_DECL(); ST_RT(62); ST();
  f32x4 acc[2][5];
#pragma unroll
  for (int b = 0; b < 2; ++b)
#pragma unroll
    for (int c = 0; c < 5; ++c) acc[b][c] = (f32x4){0.f, 0.f, 0.f, 0.f};
  const int to = (lane >> 5) * 4096 + ((lane >> 4) & 1) * 128 + ((lane >> 1) & 1) * 64 + ((lane >> 2) & 3) * 16 + (lane & 1) * 8 + wid * 512;
  const int dofs = YW ? 16384 + i16 * 128 + ((g ^ (lane & 7)) << 4) : 16384 + (lane >> 5) * 2560 + (lane & 31) * 8;
  bf16x8 TA[2][2], DA[2][5], TB[2][2], DB[2][5];
  int so = 0;
  auto rd = [&](bf16x8 (&X)[2][2], bf16x8 (&W)[2][5]) { const LAS char* st = lds + so;
#pragma unroll
    for (int h = 0; h < 2; ++h) {
#pragma unroll
      for (int b = 0; b < 2; ++b) X[h][b] = ld_tr(st + to + h * 8192 + b * 256, 2048);
#pragma unroll
      for (int c = 0; c < 5; ++c) W[h][c] = YW ? ld_row(st + (dofs ^ (h * 64)) + c * 2048) : ld_tr(st + dofs + h * 5120 + c * 256, 1280); }
    so = so + K2_STAGE == K2_LDS ? 0 : so + K2_STAGE; };
  auto mm = [&](const bf16x8 (&X)[2][2], const bf16x8 (&W)[2][5]) {
#pragma unroll
    for (int h = 0; h < 2; ++h)
#pragma unroll
      for (int c = 0; c < 5; ++c)
#pragma unroll
        for (int b = 0; b < 2; ++b) acc[b][c] = MFMA16(W[h][c], X[h][b], acc[b][c]); };
  auto ilv2 = [&]() {
    if (YW) {
#pragma unroll
      for (int i = 0; i < 18; ++i) { __builtin_amdgcn_sched_group_barrier(0x8, 1, 0); __builtin_amdgcn_sched_group_barrier(0x100, 1, 0); }
      __builtin_amdgcn_sched_group_barrier(0x8, 2, 0);
    } else {
#pragma unroll
      for (int i = 0; i < 10; ++i) { __builtin_amdgcn_sched_group_barrier(0x8, 1, 0); __builtin_amdgcn_sched_group_barrier(0x100, 2, 0); }
#pragma unroll
      for (int i = 0; i < 8; ++i) { __builtin_amdgcn_sched_group_barrier(0x8, 1, 0); __builtin_amdgcn_sched_group_barrier(0x100, 1, 0); }
      __builtin_amdgcn_sched_group_barrier(0x8, 2, 0); } };
  auto stepA = [&]() { BAR_L(); rd(TB, DB); mm(TA, DA); ilv2(); };
  auto stepB = [&]() { BAR_L(); rd(TA, DA); mm(TB, DB); ilv2(); };
  const auto yr = mk_rsrc(a.ws + WS_YX + (size_t)pair * 40960, 40960);
  char* flag = a.ws + WS_FL + (size_t)(pair * 4 + wid) * 64;
  BAR();
  ST();
  rd(TA, DA);
  SCHED();
  u32x2 dv[5]; f32x4 yv[2][5]; bool have_y = false;
  if (YW) {
#pragma unroll 1
    for (int t = 0; t < T; t += 2) { stepA(); stepB(); }
  } else {
#pragma unroll 1
    for (int t = 0; t < T - 5; t += 2) {
      stepA(); stepB(); }
#pragma unroll
    for (int c = 0; c < 5; ++c) dv[c] = *(const u32x2*)(a.dsk + 80 * dg + 16 * c + 4 * g);
    stepA();
    const unsigned long long f0 = ld_dev(flag);
    stepB(); stepA();
    if (__builtin_amdgcn_ballot_w64(f0 != epoch) == 0ull) { have_y = true;
#pragma unroll
      for (int q = 0; q < 1; ++q)
#pragma unroll
        for (int c = 0; c < 5; ++c) yv[q][c] = __builtin_bit_cast(f32x4, __builtin_amdgcn_raw_buffer_load_b128(yr, ((wid * 10 + q * 5 + c) * 64 + lane) * 16, 0, 16)); }
    stepB();
    BAR_L(); mm(TA, DA);
  }
  ST();
  if (YW) {
#pragma unroll
    for (int q = 0; q < 2; ++q)
#pragma unroll
      for (int c = 0; c < 5; ++c) st16_wt(yr, ((wid * 10 + q * 5 + c) * 64 + lane) * 16, __builtin_bit_cast(u32x4, acc[q][c]));
    wait_v<0>();
    if (lane == 0) st_dev(flag, epoch);
  } else {
    if (!have_y) {
      unsigned spins = 0; while (__builtin_amdgcn_ballot_w64(ld_dev(flag) != epoch) != 0ull && ++spins < SPIN_MAX) __builtin_amdgcn_s_sleep(1);
      asm volatile("" ::: "memory");
#pragma unroll
      for (int c = 0; c < 5; ++c) yv[0][c] = __builtin_bit_cast(f32x4, __builtin_amdgcn_raw_buffer_load_b128(yr, ((wid * 10 + c) * 64 + lane) * 16, 0, 16)); }
#pragma unroll
    for (int c = 0; c < 5; ++c) yv[1][c] = __builtin_bit_cast(f32x4, __builtin_amdgcn_raw_buffer_load_b128(yr, ((wid * 10 + 5 + c) * 64 + lane) * 16, 0, 16));
    ST();
    const LAS char* xt = lds + ((T - 5) % K2_NB) * K2_STAGE;
#pragma unroll
    for (int q = 0; q < 2; ++q) { const int row = 32 * wid + 16 * q + i16, tok = rb * 128 + row; const u32x4 rs = *(const LAS u32x4*)(xt + 20480 + row * 16);
      const float m = __uint_as_float(rs[0] << 16), r = __builtin_amdgcn_rcpf(__uint_as_float(rs[2] << 16));
#pragma unroll
      for (int c = 0; c < 5; ++c) {
        const f32x4 o = acc[q][c]; const u32x2 xv = *(const LAS u32x2*)(xt + row * 160 + c * 32 + g * 8);
        const f32x2 g01 = silu2((f32x2){o[0], o[1]}), g23 = silu2((f32x2){o[2], o[3]});
        const f32x4 xf = {bflo(xv.x), bfhi(xv.x), bflo(xv.y), bfhi(xv.y)}, df = {bflo(dv[c].x), bfhi(dv[c].x), bflo(dv[c].y), bfhi(dv[c].y)};
        const f32x4 s = yv[q][c] + (xf - m) * r * df;
        const f32x4 o4 = xf + s * (f32x4){g01.x, g01.y, g23.x, g23.y};
        *(f32x4*)(a.out + (size_t)tok * D + 80 * dg + 16 * c + 4 * g) = o4; } }
  }
  ST();
  ST(); ST_RT(63); ST_OUT(2, wid);
}

static constexpr int FUSED_LDS = K1_LDS > K2_LDS ? K1_LDS : K2_LDS;

__global__ __launch_bounds__(768, 3) void s5_block(Args a) {
  extern __shared__ __attribute__((aligned(256))) char smem[];
  LAS char* lds = (LAS char*)smem;
  const int wid = __builtin_amdgcn_readfirstlane(threadIdx.x >> 6), lane = threadIdx.x & 63;
  const int b = blockIdx.x, xcd = b & 7, idx = b >> 3, ci = idx >> 2;
  const unsigned long long epoch = mix64(ld_sc(a.ws + WS_EP));
  {
    const int rb = 4 * (xcd & 1) + (idx & 3), cq = xcd >> 1;
    const int nf = cq < 2 ? 6 : 5, f0 = cq < 2 ? 6 * cq : 12 + 5 * (cq - 2), s0 = cq < 2 ? 2 * cq : 4 + 3 * (cq - 2);
    int c32;
    if (ci < nf) { const int cg = f0 + ci; c32 = cg;
      if (wid < 4) k1_compute<false, 0>(a, lds, wid, lane, rb, cg, ci, epoch); else if (wid < 8) k1_compute<false, 1>(a, lds, wid, lane, rb, cg, ci, epoch); else k1_loader<false>(a, lds, wid - 8, lane, rb, cg, epoch); }
    else { const int cg = s0 + ci - nf; c32 = 22 + cg;
      if (wid < 4) k1_compute<true, 0>(a, lds, wid, lane, rb, cg, ci, epoch); else if (wid < 8) k1_compute<true, 1>(a, lds, wid, lane, rb, cg, ci, epoch); else k1_loader<true>(a, lds, wid - 8, lane, rb, cg, epoch); }
    asm volatile("s_waitcnt vmcnt(0) lgkmcnt(0)\n\ts_barrier" ::: "memory");
    if (wid == 4 && lane == 0) st_dev(a.ws + WS_DN + (size_t)(rb * 32 + c32) * 64, epoch);
  }
  if (wid >= 4 && wid < 8) return;
  {
    const bool yw = ci >= 4; const int rb = 4 * (xcd & 3) + (idx & 3), dg = 4 * (xcd >> 2) + (ci & 3), pair = rb * 8 + dg;
    if (yw) { if (wid < 4) k2_compute<true>(a, lds, wid, lane, rb, dg, pair, epoch); else k2_loader<true>(a, lds, wid - 8, lane, rb, dg, epoch); }
    else { if (wid < 4) k2_compute<false>(a, lds, wid, lane, rb, dg, pair, epoch); else k2_loader<false>(a, lds, wid - 8, lane, rb, dg, epoch); }
  }
}

extern "C" void kernel_launch(void* const* d_in, const int* in_sizes, int n_in, void* d_out, int out_size, void* d_ws, size_t ws_size, hipStream_t stream) {
  Args a;
  a.x = (const u16*)d_in[0]; a.lre = (const u16*)d_in[1]; a.lim = (const u16*)d_in[2]; a.bre = (const u16*)d_in[3]; a.bim = (const u16*)d_in[4];
  a.cre = (const u16*)d_in[5]; a.cim = (const u16*)d_in[6]; a.ldt = (const u16*)d_in[7]; a.dsk = (const u16*)d_in[8];
  a.w1 = (const u16*)d_in[10]; a.w3 = (const u16*)d_in[11]; a.w2 = (const u16*)d_in[12]; a.out = (float*)d_out; a.ws = (char*)d_ws;
  (void)hipFuncSetAttribute((const void*)s5_block, hipFuncAttributeMaxDynamicSharedMemorySize, FUSED_LDS);
  s5_block<<<256, 768, FUSED_LDS, stream>>>(a);
}
```

```cpp
#include <hip/hip_runtime.h>
#include <hip/hip_bf16.h>

typedef unsigned short u16;
typedef short bf16x8 __attribute__((ext_vector_type(8)));
typedef short s16x4 __attribute__((ext_vector_type(4)));
typedef float f32x4 __attribute__((ext_vector_type(4)));
typedef float f32x2 __attribute__((ext_vector_type(2)));
typedef unsigned u32x4 __attribute__((ext_vector_type(4)));
typedef unsigned u32x2 __attribute__((ext_vector_type(2)));
typedef __bf16 bf16x2_t __attribute__((ext_vector_type(2)));
#define LAS __attribute__((address_space(3)))
#define GAS __attribute__((address_space(1)))
typedef GAS unsigned gu32;
typedef GAS unsigned long long gu64;

static constexpr int L = 2048, D = 640, P = 640, F = 1728;
static constexpr size_t WS_HM = 0;
static constexpr size_t WS_HT = WS_HM + (size_t)16 * 27 * 16384;
static constexpr size_t WS_W2 = WS_HT + (size_t)16 * 20 * 16384;
static constexpr size_t WS_YX = WS_W2 + (size_t)8 * 27 * 10240;
static constexpr size_t WS_RS = WS_YX + (size_t)128 * 40960;
static constexpr size_t WS_AG = WS_RS + (size_t)L * 16;
static constexpr size_t WS_EP = WS_AG + (size_t)10 * 8 * 192 * 8;
static constexpr size_t WS_DN = WS_EP + 256;
static constexpr size_t WS_FL = WS_DN + (size_t)256 * 64;
static constexpr size_t WS_END = WS_FL + (size_t)512 * 64;
#define OUTAUX 16
static constexpr unsigned SPIN_MAX = 1u << 18;

struct Args {
  const u16* x; const u16* lre; const u16* lim; const u16* bre; const u16* bim; const u16* cre; const u16* cim;
  const u16* ldt; const u16* dsk; const u16* w1; const u16* w3; const u16* w2; float* out; char* ws;
};

__device__ __forceinline__ float bf2f(u16 b) { return __uint_as_float(((unsigned)b) << 16); }
__device__ __forceinline__ unsigned pk_bf16(float lo, float hi) { f32x2 v = {lo, hi}; bf16x2_t r = __builtin_convertvector(v, bf16x2_t); return __builtin_bit_cast(unsigned, r); }
__device__ __forceinline__ float bflo(unsigned p) { return __uint_as_float(p << 16); }
__device__ __forceinline__ float bfhi(unsigned p) { return __uint_as_float(p & 0xffff0000u); }
__device__ __forceinline__ float rbf(float f) { return bflo(pk_bf16(f, 0.f)); }
__device__ __forceinline__ float dot2(unsigned a, unsigned b, float c) { return __builtin_amdgcn_fdot2_f32_bf16(__builtin_bit_cast(bf16x2_t, a), __builtin_bit_cast(bf16x2_t, b), c, false); }
__device__ __forceinline__ f32x2 silu2(f32x2 v) { const f32x2 t = v * -1.44269504089f; f32x2 e; e.x = __builtin_amdgcn_exp2f(t.x); e.y = __builtin_amdgcn_exp2f(t.y);
  const f32x2 d = e + 1.0f; f32x2 r; r.x = __builtin_amdgcn_rcpf(d.x); r.y = __builtin_amdgcn_rcpf(d.y); return v * r; }

#define SCHED() __builtin_amdgcn_sched_barrier(0)
#define BAR() asm volatile("s_barrier" ::: "memory")
#define BAR_L() do { SCHED(); asm volatile("s_waitcnt lgkmcnt(0)\n\ts_barrier" ::: "memory"); SCHED(); } while (0)
template <int N> __device__ __forceinline__ void wait_v() { asm volatile("s_waitcnt vmcnt(%0)" :: "i"(N) : "memory"); }
#define DSPER 1
template <int ND, int NM> __device__ __forceinline__ void ilv() {
#pragma unroll
  for (int i = 0; i < (ND + DSPER - 1) / DSPER; ++i) { __builtin_amdgcn_sched_group_barrier(0x8, 1, 0); __builtin_amdgcn_sched_group_barrier(0x100, DSPER, 0); }
  __builtin_amdgcn_sched_group_barrier(0x8, ND + NM - (ND + DSPER - 1) / DSPER, 0); }
#define MFMA16(a, b, c) __builtin_amdgcn_mfma_f32_16x16x32_bf16(a, b, c, 0, 0, 0)
__device__ __forceinline__ void glds16(const void* g, LAS char* l) { __builtin_amdgcn_global_load_lds((const unsigned*)g, (LAS unsigned*)l, 16, 0, 0); }
__device__ __forceinline__ void glds16_dev(const void* g, LAS char* l) { __builtin_amdgcn_global_load_lds((const unsigned*)g, (LAS unsigned*)l, 16, 0, 16); }
__device__ __forceinline__ bf16x8 ld_row(const LAS char* p) { return *(const LAS bf16x8*)p; }
typedef short v4i16_t __attribute__((ext_vector_type(4)));
__device__ __forceinline__ s16x4 ld_tr4(const LAS char* p) { return __builtin_amdgcn_ds_read_tr16_b64_v4i16((LAS v4i16_t*)p); }
__device__ __forceinline__ bf16x8 ld_tr(const LAS char* p, int t1off) { const s16x4 lo = ld_tr4(p), hi = ld_tr4(p + t1off); return __builtin_shufflevector(lo, hi, 0, 1, 2, 3, 4, 5, 6, 7); }
#define K1AUX 16
__device__ __forceinline__ void st16_wt(__amdgpu_buffer_rsrc_t r, unsigned off, u32x4 v) { __builtin_amdgcn_raw_buffer_store_b128(v, r, off, 0, 16); }
__device__ __forceinline__ void st16_k1(__amdgpu_buffer_rsrc_t r, unsigned off, u32x4 v) { __builtin_amdgcn_raw_buffer_store_b128(v, r, off, 0, K1AUX); }
__device__ __forceinline__ void st8_wt(__amdgpu_buffer_rsrc_t r, unsigned off, u32x2 v) { __builtin_amdgcn_raw_buffer_store_b64(v, r, off, 0, 16); }
__device__ __forceinline__ void st4_wt(__amdgpu_buffer_rsrc_t r, unsigned off, unsigned v) { __builtin_amdgcn_raw_buffer_store_b32(v, r, off, 0, 16); }
__device__ __forceinline__ __amdgpu_buffer_rsrc_t mk_rsrc(const void* p, unsigned bytes) { return __builtin_amdgcn_make_buffer_rsrc((void*)p, 0, bytes, 0x00020000); }

#define ST_DECL()
#define ST()
#define STL()
#define ST_RT(slot)
#define ST_OUT(kern, wid_)

static constexpr int XSLOT = 32768, K1_WOFF = 3 * XSLOT, WSLOT = 10256, W3OFF = 5136, BSLOT = 16384, K1_RS = K1_WOFF + 6 * WSLOT  , EP_CS = K1_RS + 2048  , K1_PF = EP_CS + 640  , K1_LDS = K1_PF + 256;
static constexpr int SSM_TAB = K1_WOFF + 3 * BSLOT;
static constexpr int EP_W = 4096  , EP_G = 8192  ;

__device__ __forceinline__ unsigned long long mix64(unsigned long long z) { z += 0x9E3779B97F4A7C15ull; z = (z ^ (z >> 30)) * 0xBF58476D1CE4E5B9ull; z = (z ^ (z >> 27)) * 0x94D049BB133111EBull; return z ^ (z >> 31); }
__device__ __forceinline__ unsigned long long ld_dev(const void* p) { return __hip_atomic_load((gu64*)p, __ATOMIC_RELAXED, __HIP_MEMORY_SCOPE_AGENT); }
__device__ __forceinline__ void st_dev(void* p, unsigned long long v) { __hip_atomic_store((gu64*)p, v, __ATOMIC_RELAXED, __HIP_MEMORY_SCOPE_AGENT); }
__device__ __forceinline__ void touch(const void* g, unsigned lds_pad) { unsigned keep; asm volatile("s_mov_b32 %0, m0\n\ts_mov_b32 m0, %2\n\ts_nop 0\n\tglobal_load_lds_dword %1, off\n\ts_mov_b32 m0, %0" : "=&s"(keep) : "v"(g), "s"(lds_pad) : "memory"); }
__device__ __forceinline__ int ssm_token(int rho) { const int i = rho & 15, bb = (rho >> 4) & 3; return (rho & ~63) + 16 * (i >> 2) + 8 * (i & 1) + 2 * bb + ((i >> 1) & 1); }

__device__ __forceinline__ unsigned long long ld_sc(const void* p) { unsigned long long v; asm volatile("s_load_dwordx2 %0, %1, 0x0\n\ts_waitcnt lgkmcnt(0)" : "=s"(v) : "s"(p) : "memory"); return v; }
template <int C> __device__ __forceinline__ float dpp_add(float x) { return x + __int_as_float(__builtin_amdgcn_update_dpp(0, __float_as_int(x), C, 0xf, 0xf, true)); }
__device__ __forceinline__ float wave_sum(float x) {
  x = dpp_add<0xB1>(x); x = dpp_add<0x4E>(x); x = dpp_add<0x141>(x); x = dpp_add<0x140>(x);
  const int xi = __float_as_int(x);
  return (__int_as_float(__builtin_amdgcn_readlane(xi, 0)) + __int_as_float(__builtin_amdgcn_readlane(xi, 16))) + (__int_as_float(__builtin_amdgcn_readlane(xi, 32)) + __int_as_float(__builtin_amdgcn_readlane(xi, 48))); }

template <bool SSM>
__device__ __forceinline__ void k1_dma(const Args& a, LAS char* lds, int dw, int lane, int rb, int cg, int ci, int koff) {
  ST_DECL(); ST_RT(62); ST();
  const char* wb = (const char*)((uintptr_t)a.w1 + (dw ? (uintptr_t)a.w3 - (uintptr_t)a.w1 : 0)) + cg * 160;
  {
    const unsigned pad = (unsigned)(uintptr_t)lds + K1_PF; const int q = rb & 3, r = lane >> 1, l = lane & 1;
    if (dw == 0) touch((const char*)a.x + (size_t)(rb * 256 + 32 * ci + r) * 1280 + l * 128, pad);
    if (SSM) { if (dw == 1) touch((const char*)(q < 2 ? a.bre : a.bim) + (size_t)(cg * 64 + 32 * (q & 1) + r) * 1280 + l * 128, pad); }
    else touch(wb + (size_t)(32 * q + r) * (F * 2) + l * (cg == 21 ? 92 : 156), pad);
  }
  constexpr int NB = SSM ? 4 : 5;
  unsigned xo[8], wo[NB];
#pragma unroll
  for (int j = 0; j < 8; ++j) { const int rho = 64 * dw + 8 * j + (lane >> 3); xo[j] = ssm_token(rho) * 1280 + (((lane & 7) ^ (rho & 7)) << 4); }
  if (SSM) {
#pragma unroll
    for (int j = 0; j < 4; ++j) { const int rho = 32 * dw + 8 * j + (lane >> 3); wo[j] = rho * 1280 + (((lane & 7) ^ (rho & 7)) << 4); }
  } else { const int cmax = (F - cg * 80) - 8;
#pragma unroll
    for (int j = 0; j < 5; ++j) { const int r = 4 * j + (lane >> 4); const int ut = (r * 13) >> 6, n = r - 5 * ut;
      const int k = 16 * (ut >> 1) + 8 * ((lane >> 3) & 1) + 4 * (ut & 1) + ((lane >> 1) & 3); int col = 16 * n + 8 * (lane & 1); col = col < cmax ? col : cmax; wo[j] = k * (F * 2) + col * 2; }
  }
  const char* xb = (const char*)a.x + (size_t)rb * 256 * 1280;
  const char* bre = (const char*)a.bre + (size_t)cg * 64 * 1280; const char* bim = (const char*)a.bim + (size_t)cg * 64 * 1280;
  auto kstep = [&](int s) { return s < 2 ? s : 2 + ((s - 2 + koff) & 7); };
  auto issue_w = [&](int u) { if (!SSM) { LAS char* st = lds + K1_WOFF + dw * W3OFF + (u % 6) * WSLOT; const char* src = wb + (size_t)(2 * kstep(u >> 1) + (u & 1)) * (32 * F * 2);
#pragma unroll
      for (int q = 0; q < 5; ++q) glds16(src + wo[q], st + q * 1024); } };
  auto issue_x = [&](int j) { const int sl = (j + 5) >> 1, part = (j + 1) & 1; LAS char* st = lds + (sl % 3) * XSLOT + part * 16384 + dw * 8192; const int ko = kstep(sl) * 128; const char* src = xb + part * (128 * 1280) + ko;
#pragma unroll
    for (int q = 0; q < 8; ++q) glds16(src + xo[q], st + q * 1024);
    if (SSM) { LAS char* sb = lds + K1_WOFF + (sl % 3) * BSLOT + part * 8192 + dw * 4096; const char* sr = (part ? bim : bre) + ko;
#pragma unroll
      for (int q = 0; q < 4; ++q) glds16(sr + wo[q], sb + q * 1024); } };
#define WAITG(NWI, NXI) wait_v<(SSM ? (NXI) * 12 : (NWI) * 5 + (NXI) * 8)>()
  wait_v<0>();
  ST();
  issue_w(0); issue_x(-5); issue_x(-4); issue_w(1);
  issue_x(-3); issue_w(2);
  ST();
  WAITG(1, 1);
  ST();
  BAR();
  BAR();
  issue_x(-2); issue_w(3); issue_x(-1);
  WAITG(1, 1);
  BAR();
  issue_w(4); issue_x(0); issue_w(5);
  WAITG(2, 2);
  BAR();
  issue_w(6); issue_x(1); issue_w(7);
  WAITG(3, 1);
  BAR();
  issue_x(2); issue_w(8); issue_x(3); issue_w(9);
  WAITG(4, 3);
  BAR();
  ST();
#pragma unroll 1
  for (int j = 4; j < 14; j += 2) {
    issue_x(j); issue_w(j + 6);
    WAITG(3, 2);
    BAR();
    issue_x(j + 1); issue_w(j + 7);
    BAR();
  }
  issue_x(14); WAITG(2, 2); BAR();
  BAR();
  wait_v<0>(); BAR();
  BAR(); BAR();
  if (SSM) { BAR(); if (rb > 0) BAR(); }
  ST_RT(63); ST_OUT(1, 8 + dw);
}

template <bool SSM>
__device__ __forceinline__ void k1_aux(const Args& a, LAS char* lds, int aw, int lane, int rb, int cg, int ci, unsigned long long epoch) {
  ST_DECL(); ST_RT(62); ST();
  constexpr int NT = SSM ? 4 : 5;
  const bf16x8 ones = {0x3f80, 0x3f80, 0x3f80, 0x3f80, 0x3f80, 0x3f80, 0x3f80, 0x3f80};
  f32x4 cs[NT];
#pragma unroll
  for (int n = 0; n < NT; ++n) cs[n] = (f32x4){0.f, 0.f, 0.f, 0.f};
  const int cb = SSM ? K1_WOFF + (64 * aw + (lane & 15)) * 128 + (((lane >> 4) ^ (lane & 7)) << 4) : K1_WOFF + aw * W3OFF + (lane >> 5) * 2560 + (lane & 31) * 8;
  bf16x8 A[NT], B[NT];
  int so = 0;
  auto rd = [&](bf16x8 (&W)[NT], int h) {
#pragma unroll
    for (int n = 0; n < NT; ++n) W[n] = SSM ? ld_row(lds + so + (cb ^ (h * 64)) + n * 2048) : ld_tr(lds + so + cb + n * 256, 1280);
    if (SSM) { if (h) so = so + BSLOT == 3 * BSLOT ? 0 : so + BSLOT; } else so = so + WSLOT == 6 * WSLOT ? 0 : so + WSLOT; };
  auto mm = [&](const bf16x8 (&W)[NT]) { __builtin_amdgcn_s_setprio(3);
#pragma unroll
    for (int n = 0; n < NT; ++n) cs[n] = MFMA16(ones, W[n], cs[n]);
    SCHED(); __builtin_amdgcn_s_setprio(0); };
  const char* gp = a.ws + WS_RS + (size_t)(rb * 256) * 16; const int rho0 = 128 * aw + lane, t0 = ssm_token(rho0) * 16, t1 = ssm_token(rho0 + 64) * 16;
  unsigned so2[5];
#pragma unroll
  for (int q = 0; q < 5; ++q) { const int r = 8 * q + 4 * aw + (lane >> 4), ut = (r * 13) >> 6, n = r - 5 * ut;
    so2[q] = (16 * (ut >> 1) + 8 * ((lane >> 3) & 1) + 4 * (ut & 1) + ((lane >> 1) & 3)) * (D * 2) + (80 * rb + 16 * n + 8 * (lane & 1)) * 2; asm volatile("" : "+v"(so2[q])); }
  BAR();
  rd(A, 0);
#pragma unroll 1
  for (int j = 0; j < 16; j += 2) { BAR_L(); mm(A); rd(B, 1);
    BAR_L(); mm(B); rd(A, 0); }
  unsigned long long g0 = ld_dev(gp + t0), g1 = ld_dev(gp + t0 + 8), g2 = ld_dev(gp + t1), g3 = ld_dev(gp + t1 + 8);
  BAR_L(); mm(A); rd(B, 1);
  BAR_L(); mm(B); rd(A, 0);
  { const unsigned long long tg = epoch & 0xffffffffffffull; unsigned spins = 0;
    while (((g0 >> 16) != tg || (g1 >> 16) != tg || (g2 >> 16) != tg || (g3 >> 16) != tg) && ++spins < SPIN_MAX) { __builtin_amdgcn_s_sleep(1); g0 = ld_dev(gp + t0); g1 = ld_dev(gp + t0 + 8); g2 = ld_dev(gp + t1); g3 = ld_dev(gp + t1 + 8); }
    LAS float* rs = (LAS float*)(lds + K1_RS);
    rs[rho0] = __uint_as_float((unsigned)g0 << 16); rs[256 + rho0] = __uint_as_float((unsigned)g1 << 16); rs[rho0 + 64] = __uint_as_float((unsigned)g2 << 16); rs[256 + rho0 + 64] = __uint_as_float((unsigned)g3 << 16); }
  u32x4 w2v[5], w2w[5];
  if (!SSM) { const char* s0 = (const char*)a.w2 + (size_t)cg * (64 * D * 2); const char* s1 = (const char*)a.w2 + (size_t)(cg < 5 ? cg + 22 : cg) * (64 * D * 2);
#pragma unroll
    for (int q = 0; q < 5; ++q) { w2v[q] = *(const u32x4*)(s0 + so2[q]); w2w[q] = *(const u32x4*)(s1 + so2[q]); } }
  BAR_L(); mm(A); rd(B, 1);
  SCHED(); mm(B);
  if (SSM) { if (lane < 16) {
#pragma unroll
      for (int n = 0; n < 4; ++n) ((LAS float*)(lds + EP_CS))[64 * aw + 16 * n + lane] = cs[n][0]; } }
  else if (lane < 16) {
#pragma unroll
    for (int n = 0; n < 5; ++n) ((LAS float*)(lds + EP_CS))[80 * aw + 16 * n + lane] = cs[n][0]; }
  BAR_L();
  if (!SSM) { const auto w2r = mk_rsrc(a.ws + WS_W2 + (size_t)rb * 276480, 276480);
#pragma unroll
    for (int q = 0; q < 5; ++q) { const unsigned o = (8 * q + 4 * aw) * 256 + lane * 16; st16_k1(w2r, o + cg * 10240, w2v[q]); st16_k1(w2r, o + (cg + 22) * 10240, w2w[q]); } }
  if (SSM) { BAR(); if (rb > 0) BAR(); }
  if (SSM) {
    const int i = ((cg * 8 + rb) * 2 + aw) * 64 + lane;
    unsigned t0_, t1_; const char* p0 = (const char*)a.cre + (size_t)(i < 6400 ? i : 0) * 128; const char* p1 = (const char*)a.cim + (size_t)(i < 6400 ? i : 0) * 128;
    asm volatile("global_load_dword %0, %2, off\n\tglobal_load_dword %1, %3, off\n\ts_waitcnt vmcnt(0)" : "=&v"(t0_), "=&v"(t1_) : "v"(p0), "v"(p1) : "memory");
  }
  ST_RT(63); ST_OUT(1, 10 + aw);
}

template <bool SSM, int WN>
__device__ __forceinline__ void k1_compute(const Args& a, LAS char* lds, int wid, int lane, int rb, int cg) {
  const int wm = wid & 3, i16 = lane & 15, g = lane >> 4; constexpr int wn = WN;
  constexpr int NC = SSM ? 4 : 5;
  ST_DECL(); ST_RT(62); ST();
  const unsigned lds0 = (unsigned)(uintptr_t)lds;
  unsigned long long epoch;
  {
    const int row = rb * 256 + 8 * (SSM ? 22 + cg : cg) + wid; const unsigned* xr = (const unsigned*)(a.x + (size_t)row * D);
    unsigned v[5]; u16 t_dt = 0, t_re = 0, t_im = 0;
#pragma unroll
    for (int q = 0; q < 5; ++q) v[q] = xr[lane + 64 * q];
    if (SSM) { const int p = cg * 64 + lane; t_dt = a.ldt[p]; t_re = a.lre[p]; t_im = a.lim[p]; }
    epoch = mix64(ld_sc(a.ws + WS_EP));
    const float x0 = __uint_as_float(__builtin_amdgcn_readfirstlane(v[0]) << 16);
    float s1 = 0.f, s2 = 0.f;
#pragma unroll
    for (int q = 0; q < 5; ++q) { const float d0 = bflo(v[q]) - x0, d1 = bfhi(v[q]) - x0; s1 += d0 + d1; s2 += d0 * d0 + d1 * d1; }
    const float dm = wave_sum(s1) * (1.0f / D), var = fmaxf(wave_sum(s2) * (1.0f / D) - dm * dm, 0.f);
    const unsigned mb = pk_bf16(x0 + dm, 0.f) & 0xffff, sb = pk_bf16(__builtin_sqrtf(rbf(rbf(var) + 1e-5f)), 0.f) & 0xffff;
    if (lane < 2) st_dev(a.ws + WS_RS + (size_t)row * 16 + lane * 8, (epoch << 16) | (lane ? sb : mb));
    if (SSM && wid == 0) {
      const float dt = rbf(__expf(bf2f(t_dt))), ar = bf2f(t_re), ai = bf2f(t_im);
      const float e = __expf(dt * ar), th = dt * ai;
      const float kq = __builtin_rintf(th * 0.15915494309f), rv = __builtin_fmaf(th, 0.15915494309f, -kq) + th * 6.4206383e-9f;
      const float sn = __builtin_amdgcn_sinf(rv), cs = __builtin_amdgcn_cosf(rv);
      float pr = e * cs, pi = e * sn;
      const float nr = pr - 1.0f, ni = pi, den = 2.0f / (ar * ar + ai * ai);
      LAS f32x2* tab = (LAS f32x2*)(lds + SSM_TAB) + lane;
      tab[0] = (f32x2){pr, pi}; tab[64] = (f32x2){(nr * ar + ni * ai) * den, (ni * ar - nr * ai) * den};
#define CSQ() do { const float t_ = pr * pr - pi * pi; pi = 2.0f * pr * pi; pr = t_; } while (0)
      CSQ(); CSQ(); CSQ(); tab[128] = (f32x2){pr, pi};
      CSQ(); tab[192] = (f32x2){pr, pi}; const float ar16 = pr, ai16 = pi;
      CSQ(); tab[256] = (f32x2){pr, pi};
      tab[320] = (f32x2){pr * ar16 - pi * ai16, pr * ai16 + pi * ar16};
      CSQ(); tab[384] = (f32x2){pr, pi};
      CSQ(); CSQ(); tab[448] = (f32x2){pr, pi};
    }
  }
  f32x4 acc[4][NC];
#pragma unroll
  for (int b = 0; b < 4; ++b)
#pragma unroll
    for (int c = 0; c < NC; ++c) acc[b][c] = (f32x4){0.f, 0.f, 0.f, 0.f};
  const int xo0 = (64 * wm + i16) * 128 + ((g ^ (lane & 7)) << 4);
  int wo0, woM = 0;
  if (SSM) wo0 = K1_WOFF + (32 * wn + i16) * 128 + ((g ^ (lane & 7)) << 4);
  else { wo0 = K1_WOFF + (lane >> 5) * 2560 + (lane & 31) * 8 + wn * 768; const int p = lane & 3;
    woM = K1_WOFF + (lane >> 5) * 2560 + 512 + ((lane & 31) >> 2) * 32 + (p < 2 ? (2 * wn + p) * 8 : W3OFF + (2 * wn + p - 2) * 8); }
  bf16x8 X0[4], W0[NC], X1[4], W1[NC];
  int xs = 0, ws = 0;
  auto rd = [&](bf16x8 (&X)[4], bf16x8 (&W)[NC], int h) {
    const LAS char* xp = lds + xs + (xo0 ^ (h * 64));
    if (SSM) { const LAS char* wp = lds + (xs >> 1) + (wo0 ^ (h * 64));
      W[0] = ld_row(wp); W[1] = ld_row(wp + 2048); W[2] = ld_row(wp + 8192); W[3] = ld_row(wp + 8192 + 2048);
#pragma unroll
      for (int b = 0; b < 4; ++b) X[b] = ld_row(xp + b * 2048);
    } else { const LAS char* wp = lds + ws + wo0; const LAS char* mp = lds + ws + woM;
#pragma unroll
      for (int b = 0; b < 4; ++b) X[b] = ld_row(xp + b * 2048);
      W[0] = ld_tr(wp, 1280); W[1] = ld_tr(wp + 256, 1280); W[2] = ld_tr(wp + W3OFF, 1280); W[3] = ld_tr(wp + W3OFF + 256, 1280); W[NC - 1] = ld_tr(mp, 1280); }
    ws = ws + WSLOT == 6 * WSLOT ? 0 : ws + WSLOT;
    if (h) xs = xs + XSLOT == 3 * XSLOT ? 0 : xs + XSLOT; };
  auto mm = [&](const bf16x8 (&X)[4], const bf16x8 (&W)[NC]) {
#pragma unroll
    for (int c = 0; c < NC; ++c)
#pragma unroll
      for (int b = 0; b < 4; ++b) acc[b][c] = MFMA16(X[b], W[c], acc[b][c]); };
  constexpr int ND = SSM ? 8 : 14, NM = SSM ? 8 : 6;
  ST();
  BAR();
  ST();
  rd(X0, W0, 0);
  SCHED();
  SCHED();
#pragma unroll 1
  for (int j = 0; j < 20; j += 2) {
    BAR_L(); STL(); rd(X1, W1, 1); mm(X0, W0); ilv<ND, NM>();
    BAR_L(); STL(); rd(X0, W0, 0); mm(X1, W1); ilv<ND, NM>();
  }
  SCHED();
  ST();
  const int tid = wid * 64 + lane;
  const LAS f32x4* rst = (const LAS f32x4*)(lds + K1_RS) + 16 * wm + g;
  if constexpr (!SSM) {
    const auto hr = mk_rsrc(a.ws + WS_HM + (size_t)(2 * rb + (wm >> 1)) * (27 * 16384), 27 * 16384);
    const LAS float* csp = (const LAS float*)(lds + EP_CS);
    const bool lo = i16 < 8;
    float ng[2], nu[2];
#pragma unroll
    for (int j = 0; j < 2; ++j) { ng[j] = -csp[16 * (3 * wn + j) + i16]; nu[j] = -csp[80 + 16 * (3 * wn + j) + i16]; }
    const float nm = -csp[(lo ? 32 : 104) + 8 * wn + i16];
    f32x4 m[4], rn[4], s2[4];
#pragma unroll
    for (int b = 0; b < 4; ++b) { m[b] = rst[4 * b]; const f32x4 sd = rst[64 + 4 * b]; s2[b] = sd * sd;
#pragma unroll
      for (int q = 0; q < 4; ++q) rn[b][q] = __builtin_amdgcn_rcpf(sd[q]) * -1.44269504089f; }
    auto act = [&](f32x2 tg, f32x2 tu, f32x2 rn_, f32x2 s2_) { const f32x2 x = tg * rn_; f32x2 e; e.x = __builtin_amdgcn_exp2f(x.x); e.y = __builtin_amdgcn_exp2f(x.y); const f32x2 d = e * s2_ + s2_;
      f32x2 rc; rc.x = __builtin_amdgcn_rcpf(d.x); rc.y = __builtin_amdgcn_rcpf(d.y); return (tg * tu) * rc; };
    const unsigned nn = 4 * (wm & 1) + g, tt = (i16 >> 2) & 1, tq = nn * 512 + (i16 & 3) * 16;
#pragma unroll
    for (int j = 0; j < 2; ++j) { const int f0 = 80 * cg + 16 * (3 * wn + j); u32x4 oA, oB;
#pragma unroll
      for (int b = 0; b < 4; ++b) { const f32x4 tg = acc[b][j] + m[b] * ng[j], tu = acc[b][2 + j] + m[b] * nu[j];
        const f32x2 h01 = act((f32x2){tg[0], tg[1]}, (f32x2){tu[0], tu[1]}, (f32x2){rn[b][0], rn[b][1]}, (f32x2){s2[b][0], s2[b][1]}), h23 = act((f32x2){tg[2], tg[3]}, (f32x2){tu[2], tu[3]}, (f32x2){rn[b][2], rn[b][3]}, (f32x2){s2[b][2], s2[b][3]});
        oA[b] = pk_bf16(h01.x, h23.x); oB[b] = pk_bf16(h01.y, h23.y); }
      if (f0 < F) { const unsigned o = (f0 >> 6) * 16384 + ((f0 >> 4) & 3) * 4096 + (i16 >> 3) * 128 + tq + tt * 64; st16_k1(hr, o, oA); st16_k1(hr, (o ^ 64) + 256, oB); }
      SCHED(); }
    { u32x4 oM;
#pragma unroll
      for (int b = 0; b < 4; ++b) { const f32x4 v = acc[b][4] + m[b] * nm;
        const float s0 = lo ? v[1] : v[0], s1 = lo ? v[3] : v[2], a0 = lo ? v[0] : v[1], a1 = lo ? v[2] : v[3];
        const float q0 = __int_as_float(__builtin_amdgcn_update_dpp(0, __float_as_int(s0), 0x128, 0xf, 0xf, false)), q1 = __int_as_float(__builtin_amdgcn_update_dpp(0, __float_as_int(s1), 0x128, 0xf, 0xf, false));
        const float g0 = lo ? a0 : q0, g1 = lo ? a1 : q1, n0 = lo ? rn[b][0] : rn[b][1], n1 = lo ? rn[b][2] : rn[b][3], t0 = lo ? s2[b][0] : s2[b][1], t1 = lo ? s2[b][2] : s2[b][3];
        const float e0 = __builtin_amdgcn_exp2f(g0 * n0), e1 = __builtin_amdgcn_exp2f(g1 * n1);
        oM[b] = pk_bf16(a0 * q0 * __builtin_amdgcn_rcpf(e0 * t0 + t0), a1 * q1 * __builtin_amdgcn_rcpf(e1 * t1 + t1)); }
      const int f0 = 80 * cg + 32 + 8 * wn; st16_k1(hr, (f0 >> 6) * 16384 + ((f0 >> 4) & 3) * 4096 + ((f0 >> 3) & 1) * 128 + (i16 >> 3) * 256 + ((tt ^ (i16 >> 3)) * 64) + tq, oM); }
  } else {
    const LAS f32x2* tab = (const LAS f32x2*)(lds + SSM_TAB);
    f32x2 lam[2], l8[2];
#pragma unroll
    for (int b = 0; b < 4; ++b) { const f32x4 m = rst[4 * b], sd = rst[64 + 4 * b]; const f32x4 r = {__builtin_amdgcn_rcpf(sd[0]), __builtin_amdgcn_rcpf(sd[1]), __builtin_amdgcn_rcpf(sd[2]), __builtin_amdgcn_rcpf(sd[3])};
#pragma unroll
      for (int n = 0; n < 2; ++n) { const int ch = 32 * wn + 16 * n + i16; const float csr = -((const LAS float*)(lds + EP_CS))[ch], csi = -((const LAS float*)(lds + EP_CS))[64 + ch];
        acc[b][n] = (acc[b][n] + m * csr) * r; acc[b][2 + n] = (acc[b][2 + n] + m * csi) * r; } }
    f32x2 ea[2], ex[2], inc[2];
#pragma unroll
    for (int n = 0; n < 2; ++n) { const int ch = 32 * wn + 16 * n + i16;
      lam[n] = tab[ch]; l8[n] = tab[128 + ch]; const f32x2 l16 = tab[192 + ch], l32 = tab[256 + ch];
      const f32x2 Lr = {lam[n].x, lam[n].x}, Li = {lam[n].y, lam[n].y};
      f32x2 hr = {acc[0][n][0], acc[0][n][1]}, hi = {acc[0][2 + n][0], acc[0][2 + n][1]};
#pragma unroll
      for (int s = 1; s < 8; ++s) { const int b = s >> 1, q = s & 1; const f32x2 ur = {acc[b][n][2 * q], acc[b][n][2 * q + 1]}, ui = {acc[b][2 + n][2 * q], acc[b][2 + n][2 * q + 1]};
        const f32x2 nr = Lr * hr - Li * hi + ur, ni = Lr * hi + Li * hr + ui; hr = nr; hi = ni; }
      ea[n] = (f32x2){hr.x, hi.x};
      float er = l8[n].x * hr.x - l8[n].y * hi.x + hr.y, ei = l8[n].x * hi.x + l8[n].y * hr.x + hi.y;
      { float pr = __shfl_up(er, 16), pi = __shfl_up(ei, 16); if (g < 1) { pr = 0.f; pi = 0.f; }
        er += l16.x * pr - l16.y * pi; ei += l16.x * pi + l16.y * pr; }
      { float pr = __shfl_up(er, 32), pi = __shfl_up(ei, 32); if (g < 2) { pr = 0.f; pi = 0.f; }
        er += l32.x * pr - l32.y * pi; ei += l32.x * pi + l32.y * pr; }
      inc[n] = (f32x2){er, ei};
      { float pr = __shfl_up(er, 16), pi = __shfl_up(ei, 16); if (g < 1) { pr = 0.f; pi = 0.f; } ex[n] = (f32x2){pr, pi}; }
      if (g == 3) ((LAS f32x2*)(lds + EP_W))[wm * 64 + ch] = inc[n];
    }
    ST();
    BAR_L();
    f32x2 cw[2], pw[2];
#pragma unroll
    for (int n = 0; n < 2; ++n) { const int ch = 32 * wn + 16 * n + i16; const f32x2 l64 = tab[384 + ch];
      const f32x2 e0 = ((const LAS f32x2*)(lds + EP_W))[ch], e1 = ((const LAS f32x2*)(lds + EP_W))[64 + ch], e2 = ((const LAS f32x2*)(lds + EP_W))[128 + ch];
      float xr = 0.f, xi = 0.f, qr = 1.f, qi = 0.f;
      auto hstep = [&](f32x2 e) { const float t = l64.x * xr - l64.y * xi + e.x; xi = l64.x * xi + l64.y * xr + e.y; xr = t; const float u = l64.x * qr - l64.y * qi; qi = l64.x * qi + l64.y * qr; qr = u; };
      if (wm > 0) hstep(e0);
      if (wm > 1) hstep(e1);
      if (wm > 2) hstep(e2);
      if (wm == 3 && g == 3) { const float Ar = l64.x * xr - l64.y * xi + inc[n].x, Ai = l64.x * xi + l64.y * xr + inc[n].y;
        const unsigned long long v = ((unsigned long long)__float_as_uint(Ai) << 32) | __float_as_uint(Ar), tg = epoch << 22;
        char* gp = a.ws + WS_AG + (((size_t)cg * 8 + rb) * 192 + ch * 3) * 8;
        st_dev(gp, tg | (v & 0x3fffff)); st_dev(gp + 8, tg | ((v >> 22) & 0x3fffff)); st_dev(gp + 16, tg | (v >> 44)); }
      const f32x2 lg = g == 0 ? (f32x2){1.f, 0.f} : tab[128 + 64 * g + ch];
      cw[n] = (f32x2){ex[n].x + lg.x * xr - lg.y * xi, ex[n].y + lg.x * xi + lg.y * xr};
      pw[n] = (f32x2){lg.x * qr - lg.y * qi, lg.x * qi + lg.y * qr}; }
    const bool gat = (tid >> 6) < rb; const char* ggp = a.ws + WS_AG + (((size_t)cg * 8 + (tid >> 6)) * 192 + (tid & 63) * 3) * 8;
    unsigned long long gv0 = 0, gv1 = 0, gv2 = 0;
    if (gat) { gv0 = ld_dev(ggp); gv1 = ld_dev(ggp + 8); gv2 = ld_dev(ggp + 16); }
    ST();
#pragma unroll
    for (int n = 0; n < 2; ++n) { const f32x2 cf = tab[64 + 32 * wn + 16 * n + i16];
      const f32x2 Lr = {lam[n].x, lam[n].x}, Li = {lam[n].y, lam[n].y}, Cr = {cf.x, cf.x}, Ci = {cf.y, cf.y}, nCr = -Cr, nCi = -Ci;
      f32x2 hr = {cw[n].x, l8[n].x * cw[n].x - l8[n].y * cw[n].y + ea[n].x}, hi = {cw[n].y, l8[n].x * cw[n].y + l8[n].y * cw[n].x + ea[n].y};
#pragma unroll
      for (int s = 0; s < 8; ++s) { const int b = s >> 1, q = s & 1; const f32x2 ur = {acc[b][n][2 * q], acc[b][n][2 * q + 1]}, ui = {acc[b][2 + n][2 * q], acc[b][2 + n][2 * q + 1]};
        const f32x2 nr = Lr * hr - Li * hi + ur, ni = Lr * hi + Li * hr + ui; hr = nr; hi = ni;
        const f32x2 vr = Cr * hr + nCi * hi, vi = nCr * hi + nCi * hr;
        acc[b][n][2 * q] = vr.x; acc[b][n][2 * q + 1] = vr.y; acc[b][2 + n][2 * q] = vi.x; acc[b][2 + n][2 * q + 1] = vi.y; } }
    ST();
    if (rb > 0) {
      if (gat) { const unsigned long long tg = epoch & 0x3ffffffffffull; unsigned spins = 0;
#pragma unroll 1
        while (((gv0 >> 22) != tg || (gv1 >> 22) != tg || (gv2 >> 22) != tg) && ++spins < SPIN_MAX) { __builtin_amdgcn_s_sleep(1); gv0 = ld_dev(ggp); gv1 = ld_dev(ggp + 8); gv2 = ld_dev(ggp + 16); }
        const unsigned p0 = (unsigned)gv0 & 0x3fffff, p1 = (unsigned)gv1 & 0x3fffff, p2 = (unsigned)gv2 & 0x3fffff;
        ((LAS u32x2*)(lds + EP_G))[tid] = (u32x2){p0 | (p1 << 22), (p1 >> 10) | (p2 << 12)}; }
      ST();
      BAR_L();
      ST();
#pragma unroll
      for (int n = 0; n < 2; ++n) { const int ch = 32 * wn + 16 * n + i16; const f32x2 z = tab[448 + ch], cf = tab[64 + ch];
        float Gr = 0.f, Gi = 0.f;
        f32x2 ag[7];
#pragma unroll
        for (int rp = 0; rp < 7; ++rp) ag[rp] = ((const LAS f32x2*)(lds + EP_G))[rp * 64 + ch];
#pragma unroll
        for (int rp = 0; rp < 7; ++rp) if (rp < rb) { const float t = z.x * Gr - z.y * Gi + ag[rp].x; Gi = z.x * Gi + z.y * Gr + ag[rp].y; Gr = t; }
        const float Zr = pw[n].x * Gr - pw[n].y * Gi, Zi = pw[n].x * Gi + pw[n].y * Gr;
        const float ar = cf.x * Zr - cf.y * Zi, ai = cf.x * Zi + cf.y * Zr;
        f32x2 wr = {ar, l8[n].x * ar - l8[n].y * ai}, wi = {-ai, -(l8[n].x * ai + l8[n].y * ar)};
        const f32x2 Lr = {lam[n].x, lam[n].x}, Li = {lam[n].y, lam[n].y};
#pragma unroll
        for (int s = 0; s < 8; ++s) { const int b = s >> 1, q = s & 1; const f32x2 nr = Lr * wr + Li * wi, ni = Lr * wi - Li * wr; wr = nr; wi = ni;
          acc[b][n][2 * q] += wr.x; acc[b][n][2 * q + 1] += wr.y; acc[b][2 + n][2 * q] += wi.x; acc[b][2 + n][2 * q + 1] += wi.y; } }
    }
    const auto tr = mk_rsrc(a.ws + WS_HT + (size_t)(2 * rb + (wm >> 1)) * (20 * 16384), 20 * 16384);
#pragma unroll
    for (int n = 0; n < 2; ++n) {
      u32x4 reA, reB, imA, imB;
#pragma unroll
      for (int b = 0; b < 4; ++b) { reA[b] = pk_bf16(acc[b][n][0], acc[b][n][2]); reB[b] = pk_bf16(acc[b][n][1], acc[b][n][3]); imA[b] = pk_bf16(acc[b][2 + n][0], acc[b][2 + n][2]); imB[b] = pk_bf16(acc[b][2 + n][1], acc[b][2 + n][3]); }
      const int u = 2 * wn + n, g1 = (i16 >> 3) & 1, t = (i16 >> 2) & 1, qq = i16 & 3, nn = 4 * (wm & 1) + g;
      const unsigned bo = u * 4096 + nn * 512 + g1 * 128 + t * 64 + qq * 16 + cg * 16384, bo1 = (bo ^ 64) + 256;
      st16_k1(tr, bo, reA); st16_k1(tr, bo1, reB); st16_k1(tr, bo + 10 * 16384, imA); st16_k1(tr, bo1 + 10 * 16384, imB);
    }
  }
  ST();
  ST(); ST_RT(63); ST_OUT(1, wid);
}

static constexpr int K2_STAGE = 26624, K2_NB = 6, K2_LDS = K2_NB * K2_STAGE;

template <bool YW>
__device__ __forceinline__ void k2_loader(const Args& a, LAS char* lds, int lw, int lane, int rb, int dg, unsigned long long epoch) {
  constexpr int T = YW ? 20 : 27;
  ST_DECL(); ST_RT(62); ST();
  const char* tb = a.ws + (YW ? WS_HT + (size_t)rb * 20 * 16384 : WS_HM + (size_t)rb * 27 * 16384) + lane * 16;
  const unsigned co = YW ? (80 * dg + (lane >> 3)) * 1280 + (((lane & 7) ^ (lane >> 3)) << 4) : lane * 16; constexpr unsigned cstep = YW ? 8 * 1280 : 1024;
  auto dbase = [&](int s) { return YW ? (const char*)(s < 10 ? a.cre : a.cim) + (s % 10) * 128 : a.ws + WS_W2 + ((size_t)dg * 27 + s) * 10240; };
  const int d0 = lw < 2 ? 3 * lw : 6 + 2 * (lw - 2);
  const int koff = 0;
  auto kstep = [&](int s) { const int t = s + koff; return t >= T ? t - T : t; };
  auto issue_d = [&](int s) { LAS char* st = lds + (s % K2_NB) * K2_STAGE; const char* ds = dbase(kstep(s)) + co + d0 * cstep;
    if (YW) { glds16(ds, st + 16384 + d0 * 1024); glds16(ds + cstep, st + 16384 + (d0 + 1) * 1024); if (lw < 2) glds16(ds + 2 * cstep, st + 16384 + (d0 + 2) * 1024); }
    else { glds16_dev(ds, st + 16384 + d0 * 1024); glds16_dev(ds + cstep, st + 16384 + (d0 + 1) * 1024); if (lw < 2) glds16_dev(ds + 2 * cstep, st + 16384 + (d0 + 2) * 1024); } };
  auto issue_t = [&](int s) { LAS char* st = lds + (s % K2_NB) * K2_STAGE; const char* ts = tb + (size_t)kstep(s) * 16384 + lw * 4096;
#pragma unroll
    for (int j = 0; j < 4; ++j) glds16_dev(ts + j * 1024, st + lw * 4096 + j * 1024); };
  auto issue = [&](int s) { issue_d(s); issue_t(s); };
#define WAITK(k, ex) do { if (lw < 2) wait_v<(k) * 7 + (ex)>(); else wait_v<(k) * 6 + (ex)>(); } while (0)
  if (YW) { issue_d(0); issue_d(1); issue_d(2); issue_d(3); }
  {
    const int rbb = rb >> 1; const bool on = YW ? lane < 10 : lane < 44;
    const char* fp = a.ws + WS_DN + (size_t)(YW ? rbb * 32 + 22 + lane : (lane < 22 ? rbb * 32 + lane : dg * 32 + lane - 22)) * 64;
    unsigned spins = 0;
    while (__builtin_amdgcn_ballot_w64(on && ld_dev(on ? fp : a.ws + WS_DN) != epoch) != 0ull && ++spins < SPIN_MAX) __builtin_amdgcn_s_sleep(2);
    asm volatile("" ::: "memory"); }
  ST();
  if (YW) { issue_t(0); issue_t(1); wait_v<4>(); }
  else { issue(0); issue(1); WAITK(1, 0); }
  ST();
  BAR();
  if (YW) { issue_t(2); issue_t(3); wait_v<8>(); } else { issue(2); issue(3); WAITK(2, 0); }
  BAR();
  if (YW) { issue(4); if (lw < 2) wait_v<11>(); else wait_v<10>(); } else { issue(4); WAITK(2, 0); }
  BAR();
#pragma unroll 1
  for (int t = 1; t < T - 4; ++t) { issue(t + 4); WAITK(2, 0); BAR(); }
  if (!YW) { LAS char* st = lds + ((T - 5) % K2_NB) * K2_STAGE;
#pragma unroll
    for (int j = 0; j < 5; ++j) { const int c = (lw * 5 + j) * 64 + lane, row = (c * 6554) >> 16, ch = c - 10 * row; glds16((const char*)a.x + (size_t)(rb * 128 + row) * 1280 + dg * 160 + ch * 16, st + (lw * 5 + j) * 1024); }
    glds16_dev(a.ws + WS_RS + (size_t)(rb * 128 + (lw & 1) * 64 + lane) * 16, st + 20480 + (lw & 1) * 1024); }
  WAITK(1, YW ? 0 : 6); BAR();
  WAITK(0, YW ? 0 : 6); BAR();
  wait_v<0>(); BAR();
  if (blockIdx.x == 0 && lw == 3) {
    unsigned spins = 0; bool ok;
    do { ok = true;
#pragma unroll
      for (int q = 0; q < 4; ++q) ok &= ld_dev(a.ws + WS_DN + (size_t)(q * 64 + lane) * 64) == epoch;
    } while (__builtin_amdgcn_ballot_w64(!ok) != 0ull && ++spins < SPIN_MAX);
    if (lane == 0) st_dev(a.ws + WS_EP, epoch); }
  ST_RT(63); ST_OUT(2, 8 + lw);
}

template <bool YW>
__device__ __forceinline__ void k2_compute(const Args& a, LAS char* lds, int wid, int lane, int rb, int dg, int pair, unsigned long long epoch) {
  constexpr int T = YW ? 20 : 27;
  const int i16 = lane & 15, g = lane >> 4;
  ST_DECL(); ST_RT(62); ST();
  f32x4 acc[2][5];
#pragma unroll
  for (int b = 0; b < 2; ++b)
#pragma unroll
    for (int c = 0; c < 5; ++c) acc[b][c] = (f32x4){0.f, 0.f, 0.f, 0.f};
  const int to = (lane >> 5) * 4096 + ((lane >> 1) & 1) * (256 + 64) + ((lane >> 4) & 1) * 128 + ((lane >> 2) & 3) * 16 + (lane & 1) * 8 + wid * 1024, to1 = to ^ 64;
  const int dofs = YW ? 16384 + i16 * 128 + ((g ^ (lane & 7)) << 4) : 16384 + (lane >> 5) * 2560 + (lane & 31) * 8;
  bf16x8 TA[2][2], DA[2][5], TB[2][2], DB[2][5];
  int so = 0;
  auto rd = [&](bf16x8 (&X)[2][2], bf16x8 (&W)[2][5]) { const LAS char* st = lds + so;
#pragma unroll
    for (int h = 0; h < 2; ++h) {
#pragma unroll
      for (int b = 0; b < 2; ++b) { const s16x4 lo = ld_tr4(st + to + h * 8192 + b * 512), hi = ld_tr4(st + to1 + h * 8192 + b * 512); X[h][b] = __builtin_shufflevector(lo, hi, 0, 1, 2, 3, 4, 5, 6, 7); }
#pragma unroll
      for (int c = 0; c < 5; ++c) W[h][c] = YW ? ld_row(st + (dofs ^ (h * 64)) + c * 2048) : ld_tr(st + dofs + h * 5120 + c * 256, 1280); }
    so = so + K2_STAGE == K2_LDS ? 0 : so + K2_STAGE; };
  auto mm = [&](const bf16x8 (&X)[2][2], const bf16x8 (&W)[2][5]) {
#pragma unroll
    for (int h = 0; h < 2; ++h)
#pragma unroll
      for (int c = 0; c < 5; ++c)
#pragma unroll
        for (int b = 0; b < 2; ++b) acc[b][c] = MFMA16(W[h][c], X[h][b], acc[b][c]); };
  auto ilv2 = [&]() {
    if (YW) {
#pragma unroll
      for (int i = 0; i < 18; ++i) { __builtin_amdgcn_sched_group_barrier(0x8, 1, 0); __builtin_amdgcn_sched_group_barrier(0x100, 1, 0); }
      __builtin_amdgcn_sched_group_barrier(0x8, 2, 0);
    } else {
#pragma unroll
      for (int i = 0; i < 10; ++i) { __builtin_amdgcn_sched_group_barrier(0x8, 1, 0); __builtin_amdgcn_sched_group_barrier(0x100, 2, 0); }
#pragma unroll
      for (int i = 0; i < 8; ++i) { __builtin_amdgcn_sched_group_barrier(0x8, 1, 0); __builtin_amdgcn_sched_group_barrier(0x100, 1, 0); }
      __builtin_amdgcn_sched_group_barrier(0x8, 2, 0); } };
  auto stepA = [&]() { BAR_L(); rd(TB, DB); mm(TA, DA); ilv2(); };
  auto stepB = [&]() { BAR_L(); rd(TA, DA); mm(TB, DB); ilv2(); };
  const auto yr = mk_rsrc(a.ws + WS_YX + (size_t)pair * 40960, 40960);
  char* flag = a.ws + WS_FL + (size_t)(pair * 4 + wid) * 64;
  BAR();
  ST();
  rd(TA, DA);
  SCHED();
  u32x2 dv[5]; f32x4 yv[2][5]; bool have_y = false;
  if (YW) {
#pragma unroll 1
    for (int t = 0; t < T; t += 2) { stepA(); stepB(); }
  } else {
#pragma unroll 1
    for (int t = 0; t < T - 5; t += 2) {
      stepA(); stepB(); }
#pragma unroll
    for (int c = 0; c < 5; ++c) dv[c] = *(const u32x2*)(a.dsk + 80 * dg + 16 * c + 4 * g);
    stepA();
    const unsigned long long f0 = ld_dev(flag);
    stepB(); stepA();
    if (__builtin_amdgcn_ballot_w64(f0 != epoch) == 0ull) { have_y = true;
#pragma unroll
      for (int q = 0; q < 1; ++q)
#pragma unroll
        for (int c = 0; c < 5; ++c) yv[q][c] = __builtin_bit_cast(f32x4, __builtin_amdgcn_raw_buffer_load_b128(yr, ((wid * 10 + q * 5 + c) * 64 + lane) * 16, 0, 16)); }
    stepB();
    BAR_L(); mm(TA, DA);
  }
  ST();
  if (YW) {
#pragma unroll
    for (int q = 0; q < 2; ++q)
#pragma unroll
      for (int c = 0; c < 5; ++c) st16_wt(yr, ((wid * 10 + q * 5 + c) * 64 + lane) * 16, __builtin_bit_cast(u32x4, acc[q][c]));
    wait_v<0>();
    if (lane == 0) st_dev(flag, epoch);
  } else {
    if (!have_y) {
      unsigned spins = 0; while (__builtin_amdgcn_ballot_w64(ld_dev(flag) != epoch) != 0ull && ++spins < SPIN_MAX) __builtin_amdgcn_s_sleep(1);
      asm volatile("" ::: "memory");
#pragma unroll
      for (int c = 0; c < 5; ++c) yv[0][c] = __builtin_bit_cast(f32x4, __builtin_amdgcn_raw_buffer_load_b128(yr, ((wid * 10 + c) * 64 + lane) * 16, 0, 16)); }
#pragma unroll
    for (int c = 0; c < 5; ++c) yv[1][c] = __builtin_bit_cast(f32x4, __builtin_amdgcn_raw_buffer_load_b128(yr, ((wid * 10 + 5 + c) * 64 + lane) * 16, 0, 16));
    ST();
    const LAS char* xt = lds + ((T - 5) % K2_NB) * K2_STAGE;
    const auto orr = mk_rsrc(a.out, L * D * 4);
#pragma unroll
    for (int q = 0; q < 2; ++q) { const int row = 32 * wid + 16 * q + i16, tok = rb * 128 + row; const u32x4 rs = *(const LAS u32x4*)(xt + 20480 + row * 16);
      const float m = __uint_as_float(rs[0] << 16), r = __builtin_amdgcn_rcpf(__uint_as_float(rs[2] << 16));
#pragma unroll
      for (int c = 0; c < 5; ++c) {
        const f32x4 o = acc[q][c]; const u32x2 xv = *(const LAS u32x2*)(xt + row * 160 + c * 32 + g * 8);
        const f32x2 g01 = silu2((f32x2){o[0], o[1]}), g23 = silu2((f32x2){o[2], o[3]});
        const f32x4 xf = {bflo(xv.x), bfhi(xv.x), bflo(xv.y), bfhi(xv.y)}, df = {bflo(dv[c].x), bfhi(dv[c].x), bflo(dv[c].y), bfhi(dv[c].y)};
        const f32x4 s = yv[q][c] + (xf - m) * r * df;
        const f32x4 o4 = xf + s * (f32x4){g01.x, g01.y, g23.x, g23.y};
        __builtin_amdgcn_raw_buffer_store_b128(__builtin_bit_cast(u32x4, o4), orr, (unsigned)(tok * D + 80 * dg + 16 * c + 4 * g) * 4, 0, OUTAUX); } }
  }
  ST();
  ST(); ST_RT(63); ST_OUT(2, wid);
}

static constexpr int FUSED_LDS = K1_LDS > K2_LDS ? K1_LDS : K2_LDS;

__global__ __launch_bounds__(768, 3) void s5_block(Args a) {
  extern __shared__ __attribute__((aligned(256))) char smem[];
  LAS char* lds = (LAS char*)smem;
  const int wid = __builtin_amdgcn_readfirstlane(threadIdx.x >> 6), lane = threadIdx.x & 63;
  const int b = blockIdx.x, xcd = b & 7, idx = b >> 3, ci = idx >> 2;
#define EPOCH() mix64(ld_sc(a.ws + WS_EP))
  int dn_idx;
  {
    const int rb = 4 * (xcd & 1) + (idx & 3), cq = xcd >> 1;
    const int nf = cq < 2 ? 6 : 5, f0 = cq < 2 ? 6 * cq : 12 + 5 * (cq - 2), s0 = cq < 2 ? 2 * cq : 4 + 3 * (cq - 2);
    const int koff = (ci + 2 * (idx & 3)) & 7;
    int c32;
    if (ci < nf) { const int cg = f0 + ci; c32 = cg;
      if (wid >= 8 && wid < 10) k1_dma<false>(a, lds, wid - 8, lane, rb, cg, ci, koff);
      else if (wid < 4) k1_compute<false, 0>(a, lds, wid, lane, rb, cg); else if (wid < 8) k1_compute<false, 1>(a, lds, wid, lane, rb, cg); else k1_aux<false>(a, lds, wid - 10, lane, rb, cg, ci, EPOCH()); }
    else { const int cg = s0 + ci - nf; c32 = 22 + cg;
      if (wid >= 8 && wid < 10) k1_dma<true>(a, lds, wid - 8, lane, rb, cg, ci, koff);
      else if (wid < 4) k1_compute<true, 0>(a, lds, wid, lane, rb, cg); else if (wid < 8) k1_compute<true, 1>(a, lds, wid, lane, rb, cg); else k1_aux<true>(a, lds, wid - 10, lane, rb, cg, ci, EPOCH()); }
    dn_idx = rb * 32 + c32;
  }
  const unsigned long long epoch = EPOCH();
  asm volatile("s_waitcnt vmcnt(0) lgkmcnt(0)\n\ts_barrier" ::: "memory");
  if (wid == 4 && lane == 0) st_dev(a.ws + WS_DN + (size_t)dn_idx * 64, epoch);
  if (wid >= 4 && wid < 8) return;
  {
    const bool yw = ci >= 4; const int rb = 4 * (xcd & 3) + (idx & 3), dg = 4 * (xcd >> 2) + (ci & 3), pair = rb * 8 + dg;
    if (yw) { if (wid < 4) k2_compute<true>(a, lds, wid, lane, rb, dg, pair, epoch); else k2_loader<true>(a, lds, wid - 8, lane, rb, dg, epoch); }
    else { if (wid < 4) k2_compute<false>(a, lds, wid, lane, rb, dg, pair, epoch); else k2_loader<false>(a, lds, wid - 8, lane, rb, dg, epoch); }
  }
}

extern "C" void kernel_launch(void* const* d_in, const int* in_sizes, int n_in, void* d_out, int out_size, void* d_ws, size_t ws_size, hipStream_t stream) {
  Args a;
  a.x = (const u16*)d_in[0]; a.lre = (const u16*)d_in[1]; a.lim = (const u16*)d_in[2]; a.bre = (const u16*)d_in[3]; a.bim = (const u16*)d_in[4];
  a.cre = (const u16*)d_in[5]; a.cim = (const u16*)d_in[6]; a.ldt = (const u16*)d_in[7]; a.dsk = (const u16*)d_in[8];
  a.w1 = (const u16*)d_in[10]; a.w3 = (const u16*)d_in[11]; a.w2 = (const u16*)d_in[12]; a.out = (float*)d_out; a.ws = (char*)d_ws;
  (void)hipFuncSetAttribute((const void*)s5_block, hipFuncAttributeMaxDynamicSharedMemorySize, FUSED_LDS);
  s5_block<<<256, 768, FUSED_LDS, stream>>>(a);
}
```

```cpp
#include <hip/hip_runtime.h>
#include <hip/hip_bf16.h>

typedef unsigned short u16;
typedef short bf16x8 __attribute__((ext_vector_type(8)));
typedef short s16x4 __attribute__((ext_vector_type(4)));
typedef float f32x4 __attribute__((ext_vector_type(4)));
typedef float f32x2 __attribute__((ext_vector_type(2)));
typedef unsigned u32x4 __attribute__((ext_vector_type(4)));
typedef unsigned u32x2 __attribute__((ext_vector_type(2)));
typedef __bf16 bf16x2_t __attribute__((ext_vector_type(2)));
#define LAS __attribute__((address_space(3)))
#define GAS __attribute__((address_space(1)))
typedef GAS unsigned long long gu64;

static constexpr int L = 2048, D = 640, F = 1728;
static constexpr size_t WS_HM = 0;
static constexpr size_t WS_HT = WS_HM + (size_t)16 * 27 * 16384;
static constexpr size_t WS_W2 = WS_HT + (size_t)16 * 20 * 16384;
static constexpr size_t WS_YX = WS_W2 + (size_t)8 * 27 * 10240;
static constexpr size_t WS_RS = WS_YX + (size_t)128 * 40960;
static constexpr size_t WS_AG = WS_RS + (size_t)L * 16;
static constexpr size_t WS_EP = WS_AG + (size_t)10 * 8 * 192 * 8;
static constexpr size_t WS_DN = WS_EP + 256;
static constexpr size_t WS_FL = WS_DN + (size_t)256 * 64;
static constexpr size_t WS_END = WS_FL + (size_t)512 * 64;
static constexpr unsigned SPIN_MAX = 1u << 18;

struct Args {
  const u16* x; const u16* lre; const u16* lim; const u16* bre; const u16* bim; const u16* cre; const u16* cim;
  const u16* ldt; const u16* dsk; const u16* w1; const u16* w3; const u16* w2; float* out; char* ws;
};

__device__ __forceinline__ float bf2f(u16 b) { return __uint_as_float(((unsigned)b) << 16); }
__device__ __forceinline__ unsigned pk_bf16(float lo, float hi) { f32x2 v = {lo, hi}; bf16x2_t r = __builtin_convertvector(v, bf16x2_t); return __builtin_bit_cast(unsigned, r); }
__device__ __forceinline__ float bflo(unsigned p) { return __uint_as_float(p << 16); }
__device__ __forceinline__ float bfhi(unsigned p) { return __uint_as_float(p & 0xffff0000u); }
__device__ __forceinline__ float rbf(float f) { return bflo(pk_bf16(f, 0.f)); }
__device__ __forceinline__ f32x2 silu2(f32x2 v) { const f32x2 t = v * -1.44269504089f; f32x2 e; e.x = __builtin_amdgcn_exp2f(t.x); e.y = __builtin_amdgcn_exp2f(t.y);
  const f32x2 d = e + 1.0f; f32x2 r; r.x = __builtin_amdgcn_rcpf(d.x); r.y = __builtin_amdgcn_rcpf(d.y); return v * r; }

#define SCHED() __builtin_amdgcn_sched_barrier(0)
#define BAR() asm volatile("s_barrier" ::: "memory")
#define BAR_L() do { SCHED(); asm volatile("s_waitcnt lgkmcnt(0)\n\ts_barrier" ::: "memory"); SCHED(); } while (0)
template <int N> __device__ __forceinline__ void wait_v() { asm volatile("s_waitcnt vmcnt(%0)" :: "i"(N) : "memory"); }
template <int ND, int NM> __device__ __forceinline__ void ilv() {
#pragma unroll
  for (int i = 0; i < ND; ++i) { __builtin_amdgcn_sched_group_barrier(0x8, 1, 0); __builtin_amdgcn_sched_group_barrier(0x100, 1, 0); }
  __builtin_amdgcn_sched_group_barrier(0x8, NM, 0); }
#define MFMA16(a, b, c) __builtin_amdgcn_mfma_f32_16x16x32_bf16(a, b, c, 0, 0, 0)
__device__ __forceinline__ void glds16(const void* g, LAS char* l) { __builtin_amdgcn_global_load_lds((const unsigned*)g, (LAS unsigned*)l, 16, 0, 0); }
__device__ __forceinline__ void glds16_dev(const void* g, LAS char* l) { __builtin_amdgcn_global_load_lds((const unsigned*)g, (LAS unsigned*)l, 16, 0, 16); }
__device__ __forceinline__ bf16x8 ld_row(const LAS char* p) { return *(const LAS bf16x8*)p; }
typedef short v4i16_t __attribute__((ext_vector_type(4)));
__device__ __forceinline__ s16x4 ld_tr4(const LAS char* p) { return __builtin_amdgcn_ds_read_tr16_b64_v4i16((LAS v4i16_t*)p); }
__device__ __forceinline__ bf16x8 ld_tr(const LAS char* p, int t1off) { const s16x4 lo = ld_tr4(p), hi = ld_tr4(p + t1off); return __builtin_shufflevector(lo, hi, 0, 1, 2, 3, 4, 5, 6, 7); }
__device__ __forceinline__ void st16_wt(__amdgpu_buffer_rsrc_t r, unsigned off, u32x4 v) { __builtin_amdgcn_raw_buffer_store_b128(v, r, off, 0, 16); }
__device__ __forceinline__ __amdgpu_buffer_rsrc_t mk_rsrc(const void* p, unsigned bytes) { return __builtin_amdgcn_make_buffer_rsrc((void*)p, 0, bytes, 0x00020000); }


static constexpr int XSLOT = 32768, K1_WOFF = 3 * XSLOT, WSLOT = 10256, W3OFF = 5136, BSLOT = 16384, K1_RS = K1_WOFF + 6 * WSLOT  , EP_CS = K1_RS + 2048  , K1_PF = EP_CS + 640  , K1_LDS = K1_PF + 256;
static constexpr int SSM_TAB = K1_WOFF + 3 * BSLOT;
static constexpr int EP_W = 4096  , EP_G = 8192  ;

__device__ __forceinline__ unsigned long long mix64(unsigned long long z) { z += 0x9E3779B97F4A7C15ull; z = (z ^ (z >> 30)) * 0xBF58476D1CE4E5B9ull; z = (z ^ (z >> 27)) * 0x94D049BB133111EBull; return z ^ (z >> 31); }
__device__ __forceinline__ unsigned long long ld_dev(const void* p) { return __hip_atomic_load((gu64*)p, __ATOMIC_RELAXED, __HIP_MEMORY_SCOPE_AGENT); }
__device__ __forceinline__ void st_dev(void* p, unsigned long long v) { __hip_atomic_store((gu64*)p, v, __ATOMIC_RELAXED, __HIP_MEMORY_SCOPE_AGENT); }
__device__ __forceinline__ void touch(const void* g, unsigned lds_pad) { unsigned keep; asm volatile("s_mov_b32 %0, m0\n\ts_mov_b32 m0, %2\n\ts_nop 0\n\tglobal_load_lds_dword %1, off\n\ts_mov_b32 m0, %0" : "=&s"(keep) : "v"(g), "s"(lds_pad) : "memory"); }
__device__ __forceinline__ int ssm_token(int rho) { const int i = rho & 15, bb = (rho >> 4) & 3; return (rho & ~63) + 16 * (i >> 2) + 8 * (i & 1) + 2 * bb + ((i >> 1) & 1); }

__device__ __forceinline__ unsigned long long ld_sc(const void* p) { unsigned long long v; asm volatile("s_load_dwordx2 %0, %1, 0x0\n\ts_waitcnt lgkmcnt(0)" : "=s"(v) : "s"(p) : "memory"); return v; }
template <int C> __device__ __forceinline__ float dpp_add(float x) { return x + __int_as_float(__builtin_amdgcn_update_dpp(0, __float_as_int(x), C, 0xf, 0xf, true)); }
__device__ __forceinline__ float wave_sum(float x) {
  x = dpp_add<0xB1>(x); x = dpp_add<0x4E>(x); x = dpp_add<0x141>(x); x = dpp_add<0x140>(x);
  const int xi = __float_as_int(x);
  return (__int_as_float(__builtin_amdgcn_readlane(xi, 0)) + __int_as_float(__builtin_amdgcn_readlane(xi, 16))) + (__int_as_float(__builtin_amdgcn_readlane(xi, 32)) + __int_as_float(__builtin_amdgcn_readlane(xi, 48))); }

template <bool SSM>
__device__ __forceinline__ void k1_dma(const Args& a, LAS char* lds, int dw, int lane, int rb, int cg, int ci, int koff) {
  const char* wb = (const char*)((uintptr_t)a.w1 + (dw ? (uintptr_t)a.w3 - (uintptr_t)a.w1 : 0)) + cg * 160;
  {
    const unsigned pad = (unsigned)(uintptr_t)lds + K1_PF; const int q = rb & 3, r = lane >> 1, l = lane & 1;
    if (dw == 0) touch((const char*)a.x + (size_t)(rb * 256 + 32 * ci + r) * 1280 + l * 128, pad);
    if (SSM) { if (dw == 1) touch((const char*)(q < 2 ? a.bre : a.bim) + (size_t)(cg * 64 + 32 * (q & 1) + r) * 1280 + l * 128, pad); }
    else touch(wb + (size_t)(32 * q + r) * (F * 2) + l * (cg == 21 ? 92 : 156), pad);
  }
  constexpr int NB = SSM ? 4 : 5;
  unsigned xo[8], wo[NB];
#pragma unroll
  for (int j = 0; j < 8; ++j) { const int rho = 64 * dw + 8 * j + (lane >> 3); xo[j] = ssm_token(rho) * 1280 + (((lane & 7) ^ (rho & 7)) << 4); }
  if (SSM) {
#pragma unroll
    for (int j = 0; j < 4; ++j) { const int rho = 32 * dw + 8 * j + (lane >> 3); wo[j] = rho * 1280 + (((lane & 7) ^ (rho & 7)) << 4); }
  } else { const int cmax = (F - cg * 80) - 8;
#pragma unroll
    for (int j = 0; j < 5; ++j) { const int r = 4 * j + (lane >> 4); const int ut = (r * 13) >> 6, n = r - 5 * ut;
      const int k = 16 * (ut >> 1) + 8 * ((lane >> 3) & 1) + 4 * (ut & 1) + ((lane >> 1) & 3); int col = 16 * n + 8 * (lane & 1); col = col < cmax ? col : cmax; wo[j] = k * (F * 2) + col * 2; }
  }
  const char* xb = (const char*)a.x + (size_t)rb * 256 * 1280;
  const char* bre = (const char*)a.bre + (size_t)cg * 64 * 1280; const char* bim = (const char*)a.bim + (size_t)cg * 64 * 1280;
  auto kstep = [&](int s) { return s < 2 ? s : 2 + ((s - 2 + koff) & 7); };
  auto issue_w = [&](int u) { if (!SSM) { LAS char* st = lds + K1_WOFF + dw * W3OFF + (u % 6) * WSLOT; const char* src = wb + (size_t)(2 * kstep(u >> 1) + (u & 1)) * (32 * F * 2);
#pragma unroll
      for (int q = 0; q < 5; ++q) glds16(src + wo[q], st + q * 1024); } };
  auto issue_x = [&](int j) { const int sl = (j + 5) >> 1, part = (j + 1) & 1; LAS char* st = lds + (sl % 3) * XSLOT + part * 16384 + dw * 8192; const int ko = kstep(sl) * 128; const char* src = xb + part * (128 * 1280) + ko;
#pragma unroll
    for (int q = 0; q < 8; ++q) glds16(src + xo[q], st + q * 1024);
    if (SSM) { LAS char* sb = lds + K1_WOFF + (sl % 3) * BSLOT + part * 8192 + dw * 4096; const char* sr = (part ? bim : bre) + ko;
#pragma unroll
      for (int q = 0; q < 4; ++q) glds16(sr + wo[q], sb + q * 1024); } };
#define WAITG(NWI, NXI) wait_v<(SSM ? (NXI) * 12 : (NWI) * 5 + (NXI) * 8)>()
  wait_v<0>();
  issue_w(0); issue_x(-5); issue_x(-4); issue_w(1);
  issue_x(-3); issue_w(2);
  WAITG(1, 1);
  BAR();
  BAR();
  issue_x(-2); issue_w(3); issue_x(-1);
  WAITG(1, 1);
  BAR();
  issue_w(4); issue_x(0); issue_w(5);
  WAITG(2, 2);
  BAR();
  issue_w(6); issue_x(1); issue_w(7);
  WAITG(3, 1);
  BAR();
  issue_x(2); issue_w(8); issue_x(3); issue_w(9);
  WAITG(4, 3);
  BAR();
#pragma unroll 1
  for (int j = 4; j < 14; j += 2) {
    issue_x(j); issue_w(j + 6);
    WAITG(3, 2);
    BAR();
    issue_x(j + 1); issue_w(j + 7);
    BAR();
  }
  issue_x(14); WAITG(2, 2); BAR();
  BAR();
  wait_v<0>(); BAR();
  BAR(); BAR();
  if (SSM) { BAR(); if (rb > 0) BAR(); }
}

template <bool SSM>
__device__ __forceinline__ void k1_aux(const Args& a, LAS char* lds, int aw, int lane, int rb, int cg, int ci, unsigned long long epoch) {
  constexpr int NT = SSM ? 4 : 5;
  const bf16x8 ones = {0x3f80, 0x3f80, 0x3f80, 0x3f80, 0x3f80, 0x3f80, 0x3f80, 0x3f80};
  f32x4 cs[NT];
#pragma unroll
  for (int n = 0; n < NT; ++n) cs[n] = (f32x4){0.f, 0.f, 0.f, 0.f};
  const int cb = SSM ? K1_WOFF + (64 * aw + (lane & 15)) * 128 + (((lane >> 4) ^ (lane & 7)) << 4) : K1_WOFF + aw * W3OFF + (lane >> 5) * 2560 + (lane & 31) * 8;
  bf16x8 A[NT], B[NT];
  int so = 0;
  auto rd = [&](bf16x8 (&W)[NT], int h) {
#pragma unroll
    for (int n = 0; n < NT; ++n) W[n] = SSM ? ld_row(lds + so + (cb ^ (h * 64)) + n * 2048) : ld_tr(lds + so + cb + n * 256, 1280);
    if (SSM) { if (h) so = so + BSLOT == 3 * BSLOT ? 0 : so + BSLOT; } else so = so + WSLOT == 6 * WSLOT ? 0 : so + WSLOT; };
  auto mm = [&](const bf16x8 (&W)[NT]) { __builtin_amdgcn_s_setprio(3);
#pragma unroll
    for (int n = 0; n < NT; ++n) cs[n] = MFMA16(ones, W[n], cs[n]);
    SCHED(); __builtin_amdgcn_s_setprio(0); };
  const char* gp = a.ws + WS_RS + (size_t)(rb * 256) * 16; const int rho0 = 128 * aw + lane, t0 = ssm_token(rho0) * 16, t1 = ssm_token(rho0 + 64) * 16;
  unsigned so2[5];
#pragma unroll
  for (int q = 0; q < 5; ++q) { const int r = 8 * q + 4 * aw + (lane >> 4), ut = (r * 13) >> 6, n = r - 5 * ut;
    so2[q] = (16 * (ut >> 1) + 8 * ((lane >> 3) & 1) + 4 * (ut & 1) + ((lane >> 1) & 3)) * (D * 2) + (80 * rb + 16 * n + 8 * (lane & 1)) * 2; asm volatile("" : "+v"(so2[q])); }
  BAR();
  rd(A, 0);
#pragma unroll 1
  for (int j = 0; j < 16; j += 2) { BAR_L(); mm(A); rd(B, 1);
    BAR_L(); mm(B); rd(A, 0); }
  unsigned long long g0 = ld_dev(gp + t0), g1 = ld_dev(gp + t0 + 8), g2 = ld_dev(gp + t1), g3 = ld_dev(gp + t1 + 8);
  BAR_L(); mm(A); rd(B, 1);
  BAR_L(); mm(B); rd(A, 0);
  { const unsigned long long tg = epoch & 0xffffffffffffull; unsigned spins = 0;
    while (((g0 >> 16) != tg || (g1 >> 16) != tg || (g2 >> 16) != tg || (g3 >> 16) != tg) && ++spins < SPIN_MAX) { __builtin_amdgcn_s_sleep(1); g0 = ld_dev(gp + t0); g1 = ld_dev(gp + t0 + 8); g2 = ld_dev(gp + t1); g3 = ld_dev(gp + t1 + 8); }
    LAS float* rs = (LAS float*)(lds + K1_RS);
    rs[rho0] = __uint_as_float((unsigned)g0 << 16); rs[256 + rho0] = __uint_as_float((unsigned)g1 << 16); rs[rho0 + 64] = __uint_as_float((unsigned)g2 << 16); rs[256 + rho0 + 64] = __uint_as_float((unsigned)g3 << 16); }
  u32x4 w2v[5], w2w[5];
  if (!SSM) { const char* s0 = (const char*)a.w2 + (size_t)cg * (64 * D * 2); const char* s1 = (const char*)a.w2 + (size_t)(cg < 5 ? cg + 22 : cg) * (64 * D * 2);
#pragma unroll
    for (int q = 0; q < 5; ++q) { w2v[q] = *(const u32x4*)(s0 + so2[q]); w2w[q] = *(const u32x4*)(s1 + so2[q]); } }
  BAR_L(); mm(A); rd(B, 1);
  SCHED(); mm(B);
  if (SSM) { if (lane < 16) {
#pragma unroll
      for (int n = 0; n < 4; ++n) ((LAS float*)(lds + EP_CS))[64 * aw + 16 * n + lane] = cs[n][0]; } }
  else if (lane < 16) {
#pragma unroll
    for (int n = 0; n < 5; ++n) ((LAS float*)(lds + EP_CS))[80 * aw + 16 * n + lane] = cs[n][0]; }
  BAR_L();
  if (!SSM) { const auto w2r = mk_rsrc(a.ws + WS_W2 + (size_t)rb * 276480, 276480);
#pragma unroll
    for (int q = 0; q < 5; ++q) { const unsigned o = (8 * q + 4 * aw) * 256 + lane * 16; st16_wt(w2r, o + cg * 10240, w2v[q]); st16_wt(w2r, o + (cg + 22) * 10240, w2w[q]); } }
  if (SSM) { BAR(); if (rb > 0) BAR(); }
  if (SSM) {
    const int i = ((cg * 8 + rb) * 2 + aw) * 64 + lane;
    unsigned t0_, t1_; const char* p0 = (const char*)a.cre + (size_t)(i < 6400 ? i : 0) * 128; const char* p1 = (const char*)a.cim + (size_t)(i < 6400 ? i : 0) * 128;
    asm volatile("global_load_dword %0, %2, off\n\tglobal_load_dword %1, %3, off\n\ts_waitcnt vmcnt(0)" : "=&v"(t0_), "=&v"(t1_) : "v"(p0), "v"(p1) : "memory");
  }
}

template <bool SSM, int WN>
__device__ __forceinline__ void k1_compute(const Args& a, LAS char* lds, int wid, int lane, int rb, int cg) {
  const int wm = wid & 3, i16 = lane & 15, g = lane >> 4; constexpr int wn = WN;
  constexpr int NC = SSM ? 4 : 5;
  const unsigned lds0 = (unsigned)(uintptr_t)lds;
  unsigned long long epoch;
  {
    const int row = rb * 256 + 8 * (SSM ? 22 + cg : cg) + wid; const unsigned* xr = (const unsigned*)(a.x + (size_t)row * D);
    unsigned v[5]; u16 t_dt = 0, t_re = 0, t_im = 0;
#pragma unroll
    for (int q = 0; q < 5; ++q) v[q] = xr[lane + 64 * q];
    if (SSM) { const int p = cg * 64 + lane; t_dt = a.ldt[p]; t_re = a.lre[p]; t_im = a.lim[p]; }
    epoch = mix64(ld_sc(a.ws + WS_EP));
    const float x0 = __uint_as_float(__builtin_amdgcn_readfirstlane(v[0]) << 16);
    float s1 = 0.f, s2 = 0.f;
#pragma unroll
    for (int q = 0; q < 5; ++q) { const float d0 = bflo(v[q]) - x0, d1 = bfhi(v[q]) - x0; s1 += d0 + d1; s2 += d0 * d0 + d1 * d1; }
    const float dm = wave_sum(s1) * (1.0f / D), var = fmaxf(wave_sum(s2) * (1.0f / D) - dm * dm, 0.f);
    const unsigned mb = pk_bf16(x0 + dm, 0.f) & 0xffff, sb = pk_bf16(__builtin_sqrtf(rbf(rbf(var) + 1e-5f)), 0.f) & 0xffff;
    if (lane < 2) st_dev(a.ws + WS_RS + (size_t)row * 16 + lane * 8, (epoch << 16) | (lane ? sb : mb));
    if (SSM && wid == 0) {
      const float dt = rbf(expf(bf2f(t_dt))), ar = bf2f(t_re), ai = bf2f(t_im);
      const float e = __expf(dt * ar), th = dt * ai;
      const float kq = __builtin_rintf(th * 0.15915494309f), rv = __builtin_fmaf(th, 0.15915494309f, -kq) + th * 6.4206383e-9f;
      const float sn = __builtin_amdgcn_sinf(rv), cs = __builtin_amdgcn_cosf(rv);
      float pr = e * cs, pi = e * sn;
      const float nr = pr - 1.0f, ni = pi, den = 2.0f / (ar * ar + ai * ai);
      LAS f32x2* tab = (LAS f32x2*)(lds + SSM_TAB) + lane;
      tab[0] = (f32x2){pr, pi}; tab[64] = (f32x2){(nr * ar + ni * ai) * den, (ni * ar - nr * ai) * den};
#define CSQ() do { const float t_ = pr * pr - pi * pi; pi = 2.0f * pr * pi; pr = t_; } while (0)
      CSQ(); CSQ(); CSQ(); tab[128] = (f32x2){pr, pi};
      CSQ(); tab[192] = (f32x2){pr, pi}; const float ar16 = pr, ai16 = pi;
      CSQ(); tab[256] = (f32x2){pr, pi};
      tab[320] = (f32x2){pr * ar16 - pi * ai16, pr * ai16 + pi * ar16};
      CSQ(); tab[384] = (f32x2){pr, pi};
      CSQ(); CSQ(); tab[448] = (f32x2){pr, pi};
    }
  }
  f32x4 acc[4][NC];
#pragma unroll
  for (int b = 0; b < 4; ++b)
#pragma unroll
    for (int c = 0; c < NC; ++c) acc[b][c] = (f32x4){0.f, 0.f, 0.f, 0.f};
  const int xo0 = (64 * wm + i16) * 128 + ((g ^ (lane & 7)) << 4);
  int wo0, woM = 0;
  if (SSM) wo0 = K1_WOFF + (32 * wn + i16) * 128 + ((g ^ (lane & 7)) << 4);
  else { wo0 = K1_WOFF + (lane >> 5) * 2560 + (lane & 31) * 8 + wn * 768; const int p = lane & 3;
    woM = K1_WOFF + (lane >> 5) * 2560 + 512 + ((lane & 31) >> 2) * 32 + (p < 2 ? (2 * wn + p) * 8 : W3OFF + (2 * wn + p - 2) * 8); }
  bf16x8 X0[4], W0[NC], X1[4], W1[NC];
  int xs = 0, ws = 0;
  auto rd = [&](bf16x8 (&X)[4], bf16x8 (&W)[NC], int h) {
    const LAS char* xp = lds + xs + (xo0 ^ (h * 64));
    if (SSM) { const LAS char* wp = lds + (xs >> 1) + (wo0 ^ (h * 64));
      W[0] = ld_row(wp); W[1] = ld_row(wp + 2048); W[2] = ld_row(wp + 8192); W[3] = ld_row(wp + 8192 + 2048);
#pragma unroll
      for (int b = 0; b < 4; ++b) X[b] = ld_row(xp + b * 2048);
    } else { const LAS char* wp = lds + ws + wo0; const LAS char* mp = lds + ws + woM;
#pragma unroll
      for (int b = 0; b < 4; ++b) X[b] = ld_row(xp + b * 2048);
      W[0] = ld_tr(wp, 1280); W[1] = ld_tr(wp + 256, 1280); W[2] = ld_tr(wp + W3OFF, 1280); W[3] = ld_tr(wp + W3OFF + 256, 1280); W[NC - 1] = ld_tr(mp, 1280); }
    ws = ws + WSLOT == 6 * WSLOT ? 0 : ws + WSLOT;
    if (h) xs = xs + XSLOT == 3 * XSLOT ? 0 : xs + XSLOT; };
  auto mm = [&](const bf16x8 (&X)[4], const bf16x8 (&W)[NC]) {
#pragma unroll
    for (int c = 0; c < NC; ++c)
#pragma unroll
      for (int b = 0; b < 4; ++b) acc[b][c] = MFMA16(X[b], W[c], acc[b][c]); };
  constexpr int ND = SSM ? 8 : 14, NM = SSM ? 8 : 6;
  BAR();
  rd(X0, W0, 0);
  SCHED();
  SCHED();
#pragma unroll 1
  for (int j = 0; j < 20; j += 2) {
    BAR_L(); rd(X1, W1, 1); mm(X0, W0); ilv<ND, NM>();
    BAR_L(); rd(X0, W0, 0); mm(X1, W1); ilv<ND, NM>();
  }
  SCHED();
  const int tid = wid * 64 + lane;
  const LAS f32x4* rst = (const LAS f32x4*)(lds + K1_RS) + 16 * wm + g;
  if constexpr (!SSM) {
    const auto hr = mk_rsrc(a.ws + WS_HM + (size_t)(2 * rb + (wm >> 1)) * (27 * 16384), 27 * 16384);
    const LAS float* csp = (const LAS float*)(lds + EP_CS);
    const bool lo = i16 < 8;
    float ng[2], nu[2];
#pragma unroll
    for (int j = 0; j < 2; ++j) { ng[j] = -csp[16 * (3 * wn + j) + i16]; nu[j] = -csp[80 + 16 * (3 * wn + j) + i16]; }
    const float nm = -csp[(lo ? 32 : 104) + 8 * wn + i16];
    f32x4 m[4], rn[4], s2[4];
#pragma unroll
    for (int b = 0; b < 4; ++b) { m[b] = rst[4 * b]; const f32x4 sd = rst[64 + 4 * b]; s2[b] = sd * sd;
#pragma unroll
      for (int q = 0; q < 4; ++q) rn[b][q] = __builtin_amdgcn_rcpf(sd[q]) * -1.44269504089f; }
    auto act = [&](f32x2 tg, f32x2 tu, f32x2 rn_, f32x2 s2_) { const f32x2 x = tg * rn_; f32x2 e; e.x = __builtin_amdgcn_exp2f(x.x); e.y = __builtin_amdgcn_exp2f(x.y); const f32x2 d = e * s2_ + s2_;
      f32x2 rc; rc.x = __builtin_amdgcn_rcpf(d.x); rc.y = __builtin_amdgcn_rcpf(d.y); return (tg * tu) * rc; };
    const unsigned nn = 4 * (wm & 1) + g, tt = (i16 >> 2) & 1, tq = nn * 512 + (i16 & 3) * 16;
#pragma unroll
    for (int j = 0; j < 2; ++j) { const int f0 = 80 * cg + 16 * (3 * wn + j); u32x4 oA, oB;
#pragma unroll
      for (int b = 0; b < 4; ++b) { const f32x4 tg = acc[b][j] + m[b] * ng[j], tu = acc[b][2 + j] + m[b] * nu[j];
        const f32x2 h01 = act((f32x2){tg[0], tg[1]}, (f32x2){tu[0], tu[1]}, (f32x2){rn[b][0], rn[b][1]}, (f32x2){s2[b][0], s2[b][1]}), h23 = act((f32x2){tg[2], tg[3]}, (f32x2){tu[2], tu[3]}, (f32x2){rn[b][2], rn[b][3]}, (f32x2){s2[b][2], s2[b][3]});
        oA[b] = pk_bf16(h01.x, h23.x); oB[b] = pk_bf16(h01.y, h23.y); }
      if (f0 < F) { const unsigned o = (f0 >> 6) * 16384 + ((f0 >> 4) & 3) * 4096 + (i16 >> 3) * 128 + tq + tt * 64; st16_wt(hr, o, oA); st16_wt(hr, (o ^ 64) + 256, oB); }
      SCHED(); }
    { u32x4 oM;
#pragma unroll
      for (int b = 0; b < 4; ++b) { const f32x4 v = acc[b][4] + m[b] * nm;
        const float s0 = lo ? v[1] : v[0], s1 = lo ? v[3] : v[2], a0 = lo ? v[0] : v[1], a1 = lo ? v[2] : v[3];
        const float q0 = __int_as_float(__builtin_amdgcn_update_dpp(0, __float_as_int(s0), 0x128, 0xf, 0xf, false)), q1 = __int_as_float(__builtin_amdgcn_update_dpp(0, __float_as_int(s1), 0x128, 0xf, 0xf, false));
        const float g0 = lo ? a0 : q0, g1 = lo ? a1 : q1, n0 = lo ? rn[b][0] : rn[b][1], n1 = lo ? rn[b][2] : rn[b][3], t0 = lo ? s2[b][0] : s2[b][1], t1 = lo ? s2[b][2] : s2[b][3];
        const float e0 = __builtin_amdgcn_exp2f(g0 * n0), e1 = __builtin_amdgcn_exp2f(g1 * n1);
        oM[b] = pk_bf16(a0 * q0 * __builtin_amdgcn_rcpf(e0 * t0 + t0), a1 * q1 * __builtin_amdgcn_rcpf(e1 * t1 + t1)); }
      const int f0 = 80 * cg + 32 + 8 * wn; st16_wt(hr, (f0 >> 6) * 16384 + ((f0 >> 4) & 3) * 4096 + ((f0 >> 3) & 1) * 128 + (i16 >> 3) * 256 + ((tt ^ (i16 >> 3)) * 64) + tq, oM); }
  } else {
    const LAS f32x2* tab = (const LAS f32x2*)(lds + SSM_TAB);
    f32x2 lam[2], l8[2];
#pragma unroll
    for (int b = 0; b < 4; ++b) { const f32x4 m = rst[4 * b], sd = rst[64 + 4 * b]; const f32x4 r = {__builtin_amdgcn_rcpf(sd[0]), __builtin_amdgcn_rcpf(sd[1]), __builtin_amdgcn_rcpf(sd[2]), __builtin_amdgcn_rcpf(sd[3])};
#pragma unroll
      for (int n = 0; n < 2; ++n) { const int ch = 32 * wn + 16 * n + i16; const float csr = -((const LAS float*)(lds + EP_CS))[ch], csi = -((const LAS float*)(lds + EP_CS))[64 + ch];
        acc[b][n] = (acc[b][n] + m * csr) * r; acc[b][2 + n] = (acc[b][2 + n] + m * csi) * r; } }
    f32x2 ea[2], ex[2], inc[2];
#pragma unroll
    for (int n = 0; n < 2; ++n) { const int ch = 32 * wn + 16 * n + i16;
      lam[n] = tab[ch]; l8[n] = tab[128 + ch]; const f32x2 l16 = tab[192 + ch], l32 = tab[256 + ch];
      const f32x2 Lr = {lam[n].x, lam[n].x}, Li = {lam[n].y, lam[n].y};
      f32x2 hr = {acc[0][n][0], acc[0][n][1]}, hi = {acc[0][2 + n][0], acc[0][2 + n][1]};
#pragma unroll
      for (int s = 1; s < 8; ++s) { const int b = s >> 1, q = s & 1; const f32x2 ur = {acc[b][n][2 * q], acc[b][n][2 * q + 1]}, ui = {acc[b][2 + n][2 * q], acc[b][2 + n][2 * q + 1]};
        const f32x2 nr = Lr * hr - Li * hi + ur, ni = Lr * hi + Li * hr + ui; hr = nr; hi = ni; }
      ea[n] = (f32x2){hr.x, hi.x};
      float er = l8[n].x * hr.x - l8[n].y * hi.x + hr.y, ei = l8[n].x * hi.x + l8[n].y * hr.x + hi.y;
      { float pr = __shfl_up(er, 16), pi = __shfl_up(ei, 16); if (g < 1) { pr = 0.f; pi = 0.f; }
        er += l16.x * pr - l16.y * pi; ei += l16.x * pi + l16.y * pr; }
      { float pr = __shfl_up(er, 32), pi = __shfl_up(ei, 32); if (g < 2) { pr = 0.f; pi = 0.f; }
        er += l32.x * pr - l32.y * pi; ei += l32.x * pi + l32.y * pr; }
      inc[n] = (f32x2){er, ei};
      { float pr = __shfl_up(er, 16), pi = __shfl_up(ei, 16); if (g < 1) { pr = 0.f; pi = 0.f; } ex[n] = (f32x2){pr, pi}; }
      if (g == 3) ((LAS f32x2*)(lds + EP_W))[wm * 64 + ch] = inc[n];
    }
    BAR_L();
    f32x2 cw[2], pw[2];
#pragma unroll
    for (int n = 0; n < 2; ++n) { const int ch = 32 * wn + 16 * n + i16; const f32x2 l64 = tab[384 + ch];
      const f32x2 e0 = ((const LAS f32x2*)(lds + EP_W))[ch], e1 = ((const LAS f32x2*)(lds + EP_W))[64 + ch], e2 = ((const LAS f32x2*)(lds + EP_W))[128 + ch];
      float xr = 0.f, xi = 0.f, qr = 1.f, qi = 0.f;
      auto hstep = [&](f32x2 e) { const float t = l64.x * xr - l64.y * xi + e.x; xi = l64.x * xi + l64.y * xr + e.y; xr = t; const float u = l64.x * qr - l64.y * qi; qi = l64.x * qi + l64.y * qr; qr = u; };
      if (wm > 0) hstep(e0);
      if (wm > 1) hstep(e1);
      if (wm > 2) hstep(e2);
      if (wm == 3 && g == 3) { const float Ar = l64.x * xr - l64.y * xi + inc[n].x, Ai = l64.x * xi + l64.y * xr + inc[n].y;
        const unsigned long long v = ((unsigned long long)__float_as_uint(Ai) << 32) | __float_as_uint(Ar), tg = epoch << 22;
        char* gp = a.ws + WS_AG + (((size_t)cg * 8 + rb) * 192 + ch * 3) * 8;
        st_dev(gp, tg | (v & 0x3fffff)); st_dev(gp + 8, tg | ((v >> 22) & 0x3fffff)); st_dev(gp + 16, tg | (v >> 44)); }
      const f32x2 lg = g == 0 ? (f32x2){1.f, 0.f} : tab[128 + 64 * g + ch];
      cw[n] = (f32x2){ex[n].x + lg.x * xr - lg.y * xi, ex[n].y + lg.x * xi + lg.y * xr};
      pw[n] = (f32x2){lg.x * qr - lg.y * qi, lg.x * qi + lg.y * qr}; }
    const bool gat = (tid >> 6) < rb; const char* ggp = a.ws + WS_AG + (((size_t)cg * 8 + (tid >> 6)) * 192 + (tid & 63) * 3) * 8;
    unsigned long long gv0 = 0, gv1 = 0, gv2 = 0;
    if (gat) { gv0 = ld_dev(ggp); gv1 = ld_dev(ggp + 8); gv2 = ld_dev(ggp + 16); }
#pragma unroll
    for (int n = 0; n < 2; ++n) { const f32x2 cf = tab[64 + 32 * wn + 16 * n + i16];
      const f32x2 Lr = {lam[n].x, lam[n].x}, Li = {lam[n].y, lam[n].y}, Cr = {cf.x, cf.x}, Ci = {cf.y, cf.y}, nCr = -Cr, nCi = -Ci;
      f32x2 hr = {cw[n].x, l8[n].x * cw[n].x - l8[n].y * cw[n].y + ea[n].x}, hi = {cw[n].y, l8[n].x * cw[n].y + l8[n].y * cw[n].x + ea[n].y};
#pragma unroll
      for (int s = 0; s < 8; ++s) { const int b = s >> 1, q = s & 1; const f32x2 ur = {acc[b][n][2 * q], acc[b][n][2 * q + 1]}, ui = {acc[b][2 + n][2 * q], acc[b][2 + n][2 * q + 1]};
        const f32x2 nr = Lr * hr - Li * hi + ur, ni = Lr * hi + Li * hr + ui; hr = nr; hi = ni;
        const f32x2 vr = Cr * hr + nCi * hi, vi = nCr * hi + nCi * hr;
        acc[b][n][2 * q] = vr.x; acc[b][n][2 * q + 1] = vr.y; acc[b][2 + n][2 * q] = vi.x; acc[b][2 + n][2 * q + 1] = vi.y; } }
    if (rb > 0) {
      if (gat) { const unsigned long long tg = epoch & 0x3ffffffffffull; unsigned spins = 0;
#pragma unroll 1
        while (((gv0 >> 22) != tg || (gv1 >> 22) != tg || (gv2 >> 22) != tg) && ++spins < SPIN_MAX) { __builtin_amdgcn_s_sleep(1); gv0 = ld_dev(ggp); gv1 = ld_dev(ggp + 8); gv2 = ld_dev(ggp + 16); }
        const unsigned p0 = (unsigned)gv0 & 0x3fffff, p1 = (unsigned)gv1 & 0x3fffff, p2 = (unsigned)gv2 & 0x3fffff;
        ((LAS u32x2*)(lds + EP_G))[tid] = (u32x2){p0 | (p1 << 22), (p1 >> 10) | (p2 << 12)}; }
      BAR_L();
#pragma unroll
      for (int n = 0; n < 2; ++n) { const int ch = 32 * wn + 16 * n + i16; const f32x2 z = tab[448 + ch], cf = tab[64 + ch];
        float Gr = 0.f, Gi = 0.f;
        f32x2 ag[7];
#pragma unroll
        for (int rp = 0; rp < 7; ++rp) ag[rp] = ((const LAS f32x2*)(lds + EP_G))[rp * 64 + ch];
#pragma unroll
        for (int rp = 0; rp < 7; ++rp) if (rp < rb) { const float t = z.x * Gr - z.y * Gi + ag[rp].x; Gi = z.x * Gi + z.y * Gr + ag[rp].y; Gr = t; }
        const float Zr = pw[n].x * Gr - pw[n].y * Gi, Zi = pw[n].x * Gi + pw[n].y * Gr;
        const float ar = cf.x * Zr - cf.y * Zi, ai = cf.x * Zi + cf.y * Zr;
        f32x2 wr = {ar, l8[n].x * ar - l8[n].y * ai}, wi = {-ai, -(l8[n].x * ai + l8[n].y * ar)};
        const f32x2 Lr = {lam[n].x, lam[n].x}, Li = {lam[n].y, lam[n].y};
#pragma unroll
        for (int s = 0; s < 8; ++s) { const int b = s >> 1, q = s & 1; const f32x2 nr = Lr * wr + Li * wi, ni = Lr * wi - Li * wr; wr = nr; wi = ni;
          acc[b][n][2 * q] += wr.x; acc[b][n][2 * q + 1] += wr.y; acc[b][2 + n][2 * q] += wi.x; acc[b][2 + n][2 * q + 1] += wi.y; } }
    }
    const auto tr = mk_rsrc(a.ws + WS_HT + (size_t)(2 * rb + (wm >> 1)) * (20 * 16384), 20 * 16384);
#pragma unroll
    for (int n = 0; n < 2; ++n) {
      u32x4 reA, reB, imA, imB;
#pragma unroll
      for (int b = 0; b < 4; ++b) { reA[b] = pk_bf16(acc[b][n][0], acc[b][n][2]); reB[b] = pk_bf16(acc[b][n][1], acc[b][n][3]); imA[b] = pk_bf16(acc[b][2 + n][0], acc[b][2 + n][2]); imB[b] = pk_bf16(acc[b][2 + n][1], acc[b][2 + n][3]); }
      const int u = 2 * wn + n, g1 = (i16 >> 3) & 1, t = (i16 >> 2) & 1, qq = i16 & 3, nn = 4 * (wm & 1) + g;
      const unsigned bo = u * 4096 + nn * 512 + g1 * 128 + t * 64 + qq * 16 + cg * 16384, bo1 = (bo ^ 64) + 256;
      st16_wt(tr, bo, reA); st16_wt(tr, bo1, reB); st16_wt(tr, bo + 10 * 16384, imA); st16_wt(tr, bo1 + 10 * 16384, imB);
    }
  }
}

static constexpr int K2_STAGE = 26624, K2_NB = 5, K2_RING = K2_NB * K2_STAGE, K2_XT = K2_RING  , K2_LDS = K2_XT + 22784;

template <bool YW>
__device__ __forceinline__ void k2_loader(const Args& a, LAS char* lds, int lw, int lane, int rb, int dg, unsigned long long epoch) {
  constexpr int T = YW ? 20 : 27;
  const char* tb = a.ws + (YW ? WS_HT + (size_t)rb * 20 * 16384 : WS_HM + (size_t)rb * 27 * 16384) + lane * 16;
  const unsigned co = YW ? (80 * dg + (lane >> 3)) * 1280 + (((lane & 7) ^ (lane >> 3)) << 4) : lane * 16; constexpr unsigned cstep = YW ? 8 * 1280 : 1024;
  auto dbase = [&](int s) { return YW ? (const char*)(s < 10 ? a.cre : a.cim) + (s % 10) * 128 : a.ws + WS_W2 + ((size_t)dg * 27 + s) * 10240; };
  const int d0 = lw < 2 ? 3 * lw : 6 + 2 * (lw - 2);
  auto issue_d = [&](int s) { LAS char* st = lds + (s % K2_NB) * K2_STAGE; const char* ds = dbase(s) + co + d0 * cstep;
    if (YW) { glds16(ds, st + 16384 + d0 * 1024); glds16(ds + cstep, st + 16384 + (d0 + 1) * 1024); if (lw < 2) glds16(ds + 2 * cstep, st + 16384 + (d0 + 2) * 1024); }
    else { glds16_dev(ds, st + 16384 + d0 * 1024); glds16_dev(ds + cstep, st + 16384 + (d0 + 1) * 1024); if (lw < 2) glds16_dev(ds + 2 * cstep, st + 16384 + (d0 + 2) * 1024); } };
  auto issue_t = [&](int s) { LAS char* st = lds + (s % K2_NB) * K2_STAGE; const char* ts = tb + (size_t)s * 16384 + lw * 4096;
#pragma unroll
    for (int j = 0; j < 4; ++j) glds16_dev(ts + j * 1024, st + lw * 4096 + j * 1024); };
  auto issue = [&](int s) { issue_d(s); issue_t(s); };
#define WAITK(k, ex) do { if (lw < 2) wait_v<(k) * 7 + (ex)>(); else wait_v<(k) * 6 + (ex)>(); } while (0)
  if (YW) { issue_d(0); issue_d(1); issue_d(2); issue_d(3); }
  else {
#pragma unroll
    for (int j = 0; j < 5; ++j) { const int c = (lw * 5 + j) * 64 + lane, row = (c * 6554) >> 16, ch = c - 10 * row; glds16((const char*)a.x + (size_t)(rb * 128 + row) * 1280 + dg * 160 + ch * 16, lds + K2_XT + (lw * 5 + j) * 1024); }
    if (lw == 0 && lane < 10) glds16((const char*)a.dsk + dg * 160 + lane * 16, lds + K2_XT + 22528); }
  {
    const int rbb = rb >> 1; const bool on = YW ? lane < 10 : lane < 44;
    const char* fp = a.ws + WS_DN + (size_t)(YW ? rbb * 32 + 22 + lane : (lane < 22 ? rbb * 32 + lane : dg * 32 + lane - 22)) * 64;
    unsigned spins = 0;
    while (__builtin_amdgcn_ballot_w64(on && ld_dev(on ? fp : a.ws + WS_DN) != epoch) != 0ull && ++spins < SPIN_MAX) __builtin_amdgcn_s_sleep(2);
    asm volatile("" ::: "memory"); }
  if (YW) { issue_t(0); issue_t(1); wait_v<4>(); }
  else { glds16_dev(a.ws + WS_RS + (size_t)(rb * 128 + (lw & 1) * 64 + lane) * 16, lds + K2_XT + 20480 + (lw & 1) * 1024); issue(0); issue(1); WAITK(1, 0); }
  BAR();
  if (YW) { issue_t(2); issue_t(3); wait_v<8>(); } else { issue(2); issue(3); WAITK(2, 0); }
  BAR();
  if (YW) { issue(4); if (lw < 2) wait_v<11>(); else wait_v<10>(); } else { issue(4); WAITK(2, 0); }
  BAR();
#pragma unroll 1
  for (int t = 1; t < T - 4; ++t) { issue(t + 4); WAITK(2, 0); BAR(); }
  WAITK(1, 0); BAR();
  wait_v<0>(); BAR();
  BAR();
  if (blockIdx.x == 0 && lw == 3) {
    unsigned spins = 0; bool ok;
    do { ok = true;
#pragma unroll
      for (int q = 0; q < 4; ++q) ok &= ld_dev(a.ws + WS_DN + (size_t)(q * 64 + lane) * 64) == epoch;
    } while (__builtin_amdgcn_ballot_w64(!ok) != 0ull && ++spins < SPIN_MAX);
    if (lane == 0) st_dev(a.ws + WS_EP, epoch); }
}

template <bool YW>
__device__ __forceinline__ void k2_compute(const Args& a, LAS char* lds, int wid, int lane, int rb, int dg, int pair, unsigned long long epoch) {
  constexpr int T = YW ? 20 : 27;
  const int i16 = lane & 15, g = lane >> 4;
  f32x4 acc[2][5];
#pragma unroll
  for (int b = 0; b < 2; ++b)
#pragma unroll
    for (int c = 0; c < 5; ++c) acc[b][c] = (f32x4){0.f, 0.f, 0.f, 0.f};
  const int to = (lane >> 5) * 4096 + ((lane >> 1) & 1) * (256 + 64) + ((lane >> 4) & 1) * 128 + ((lane >> 2) & 3) * 16 + (lane & 1) * 8 + wid * 1024, to1 = to ^ 64;
  const int dofs = YW ? 16384 + i16 * 128 + ((g ^ (lane & 7)) << 4) : 16384 + (lane >> 5) * 2560 + (lane & 31) * 8;
  bf16x8 TA[2][2], DA[2][5], TB[2][2], DB[2][5];
  int so = 0;
  auto rd = [&](bf16x8 (&X)[2][2], bf16x8 (&W)[2][5]) { const LAS char* st = lds + so;
#pragma unroll
    for (int h = 0; h < 2; ++h) {
#pragma unroll
      for (int b = 0; b < 2; ++b) { const s16x4 lo = ld_tr4(st + to + h * 8192 + b * 512), hi = ld_tr4(st + to1 + h * 8192 + b * 512); X[h][b] = __builtin_shufflevector(lo, hi, 0, 1, 2, 3, 4, 5, 6, 7); }
#pragma unroll
      for (int c = 0; c < 5; ++c) W[h][c] = YW ? ld_row(st + (dofs ^ (h * 64)) + c * 2048) : ld_tr(st + dofs + h * 5120 + c * 256, 1280); }
    so = so + K2_STAGE == K2_RING ? 0 : so + K2_STAGE; };
  auto mm = [&](const bf16x8 (&X)[2][2], const bf16x8 (&W)[2][5]) {
#pragma unroll
    for (int h = 0; h < 2; ++h)
#pragma unroll
      for (int c = 0; c < 5; ++c)
#pragma unroll
        for (int b = 0; b < 2; ++b) acc[b][c] = MFMA16(W[h][c], X[h][b], acc[b][c]); };
  auto ilv2 = [&]() {
    if (YW) {
#pragma unroll
      for (int i = 0; i < 18; ++i) { __builtin_amdgcn_sched_group_barrier(0x8, 1, 0); __builtin_amdgcn_sched_group_barrier(0x100, 1, 0); }
      __builtin_amdgcn_sched_group_barrier(0x8, 2, 0);
    } else {
#pragma unroll
      for (int i = 0; i < 10; ++i) { __builtin_amdgcn_sched_group_barrier(0x8, 1, 0); __builtin_amdgcn_sched_group_barrier(0x100, 2, 0); }
#pragma unroll
      for (int i = 0; i < 8; ++i) { __builtin_amdgcn_sched_group_barrier(0x8, 1, 0); __builtin_amdgcn_sched_group_barrier(0x100, 1, 0); }
      __builtin_amdgcn_sched_group_barrier(0x8, 2, 0); } };
  auto stepA = [&]() { BAR_L(); rd(TB, DB); mm(TA, DA); ilv2(); };
  auto stepB = [&]() { BAR_L(); rd(TA, DA); mm(TB, DB); ilv2(); };
  const auto yr = mk_rsrc(a.ws + WS_YX + (size_t)pair * 40960, 40960);
#define YOFF(q, c) ((((wid * 10 + (q) * 5 + (c)) * 64) + lane) * 16)
  char* flag = a.ws + WS_FL + (size_t)(pair * 4 + wid) * 64;
  BAR();
  rd(TA, DA);
  SCHED();
  f32x4 yv[2][5]; bool have_y = false;
  if (YW) {
#pragma unroll 1
    for (int t = 0; t < T; t += 2) { stepA(); stepB(); }
  } else {
#pragma unroll 1
    for (int t = 0; t < T - 5; t += 2) {
      stepA(); stepB(); }
    stepA();
    const unsigned long long f0 = ld_dev(flag);
    stepB(); stepA();
    if (__builtin_amdgcn_ballot_w64(f0 != epoch) == 0ull) { have_y = true;
#pragma unroll
      for (int q = 0; q < 1; ++q)
#pragma unroll
        for (int c = 0; c < 5; ++c) yv[q][c] = __builtin_bit_cast(f32x4, __builtin_amdgcn_raw_buffer_load_b128(yr, YOFF(q, c), 0, 16)); }
    stepB();
    BAR_L(); mm(TA, DA);
  }
  if (YW) {
#pragma unroll
    for (int q = 0; q < 2; ++q)
#pragma unroll
      for (int c = 0; c < 5; ++c) st16_wt(yr, YOFF(q, c), __builtin_bit_cast(u32x4, acc[q][c]));
    wait_v<0>();
    if (lane == 0) st_dev(flag, epoch);
  } else {
    if (!have_y) {
      unsigned spins = 0; while (__builtin_amdgcn_ballot_w64(ld_dev(flag) != epoch) != 0ull && ++spins < SPIN_MAX) __builtin_amdgcn_s_sleep(1);
      asm volatile("" ::: "memory");
#pragma unroll
      for (int c = 0; c < 5; ++c) yv[0][c] = __builtin_bit_cast(f32x4, __builtin_amdgcn_raw_buffer_load_b128(yr, YOFF(0, c), 0, 16)); }
#pragma unroll
    for (int c = 0; c < 5; ++c) yv[1][c] = __builtin_bit_cast(f32x4, __builtin_amdgcn_raw_buffer_load_b128(yr, YOFF(1, c), 0, 16));
    const LAS char* xt = lds + K2_XT;
    const auto orr = mk_rsrc(a.out, L * D * 4);
#pragma unroll
    for (int q = 0; q < 2; ++q) { const int row = 32 * wid + 16 * q + i16, tok = rb * 128 + row; const u32x4 rs = *(const LAS u32x4*)(xt + 20480 + row * 16);
      const float m = __uint_as_float(rs[0] << 16), r = __builtin_amdgcn_rcpf(__uint_as_float(rs[2] << 16));
#pragma unroll
      for (int c = 0; c < 5; ++c) {
        const f32x4 o = acc[q][c]; const u32x2 xv = *(const LAS u32x2*)(xt + row * 160 + c * 32 + g * 8), dvc = *(const LAS u32x2*)(xt + 22528 + c * 32 + g * 8);
        const f32x2 g01 = silu2((f32x2){o[0], o[1]}), g23 = silu2((f32x2){o[2], o[3]});
        const f32x4 xf = {bflo(xv.x), bfhi(xv.x), bflo(xv.y), bfhi(xv.y)}, df = {bflo(dvc.x), bfhi(dvc.x), bflo(dvc.y), bfhi(dvc.y)};
        const f32x4 s = yv[q][c] + (xf - m) * r * df;
        const f32x4 o4 = xf + s * (f32x4){g01.x, g01.y, g23.x, g23.y};
        st16_wt(orr, (unsigned)(tok * D + 80 * dg + 16 * c + 4 * g) * 4, __builtin_bit_cast(u32x4, o4)); } }
  }
}

static constexpr int FUSED_LDS = K1_LDS > K2_LDS ? K1_LDS : K2_LDS;

__global__ __launch_bounds__(768, 3) void s5_block(Args a) {
  extern __shared__ __attribute__((aligned(256))) char smem[];
  LAS char* lds = (LAS char*)smem;
  const int wid = __builtin_amdgcn_readfirstlane(threadIdx.x >> 6), lane = threadIdx.x & 63;
  const int b = blockIdx.x, xcd = b & 7, idx = b >> 3, ci = idx >> 2;
#define EPOCH() mix64(ld_sc(a.ws + WS_EP))
  int dn_idx;
  {
    const int rb = 4 * (xcd & 1) + (idx & 3), cq = xcd >> 1;
    const int nf = cq < 2 ? 6 : 5, f0 = cq < 2 ? 6 * cq : 12 + 5 * (cq - 2), s0 = cq < 2 ? 2 * cq : 4 + 3 * (cq - 2);
    const int koff = (ci + 2 * (idx & 3)) & 7;
    int c32;
    if (ci < nf) { const int cg = f0 + ci; c32 = cg;
      if (wid >= 8 && wid < 10) k1_dma<false>(a, lds, wid - 8, lane, rb, cg, ci, koff);
      else if (wid < 4) k1_compute<false, 0>(a, lds, wid, lane, rb, cg); else if (wid < 8) k1_compute<false, 1>(a, lds, wid, lane, rb, cg); else k1_aux<false>(a, lds, wid - 10, lane, rb, cg, ci, EPOCH()); }
    else { const int cg = s0 + ci - nf; c32 = 22 + cg;
      if (wid >= 8 && wid < 10) k1_dma<true>(a, lds, wid - 8, lane, rb, cg, ci, koff);
      else if (wid < 4) k1_compute<true, 0>(a, lds, wid, lane, rb, cg); else if (wid < 8) k1_compute<true, 1>(a, lds, wid, lane, rb, cg); else k1_aux<true>(a, lds, wid - 10, lane, rb, cg, ci, EPOCH()); }
    dn_idx = rb * 32 + c32;
  }
  const unsigned long long epoch = EPOCH();
  asm volatile("s_waitcnt vmcnt(0) lgkmcnt(0)\n\ts_barrier" ::: "memory");
  if (wid == 4 && lane == 0) st_dev(a.ws + WS_DN + (size_t)dn_idx * 64, epoch);
  if (wid >= 4 && wid < 8) return;
  {
    const bool yw = ci >= 4; const int rb = 4 * (xcd & 3) + (idx & 3), dg = 4 * (xcd >> 2) + (ci & 3), pair = rb * 8 + dg;
    if (yw) { if (wid < 4) k2_compute<true>(a, lds, wid, lane, rb, dg, pair, epoch); else k2_loader<true>(a, lds, wid - 8, lane, rb, dg, epoch); }
    else { if (wid < 4) k2_compute<false>(a, lds, wid, lane, rb, dg, pair, epoch); else k2_loader<false>(a, lds, wid - 8, lane, rb, dg, epoch); }
  }
}

extern "C" void kernel_launch(void* const* d_in, const int* in_sizes, int n_in, void* d_out, int out_size, void* d_ws, size_t ws_size, hipStream_t stream) {
  Args a;
  a.x = (const u16*)d_in[0]; a.lre = (const u16*)d_in[1]; a.lim = (const u16*)d_in[2]; a.bre = (const u16*)d_in[3]; a.bim = (const u16*)d_in[4];
  a.cre = (const u16*)d_in[5]; a.cim = (const u16*)d_in[6]; a.ldt = (const u16*)d_in[7]; a.dsk = (const u16*)d_in[8];
  a.w1 = (const u16*)d_in[10]; a.w3 = (const u16*)d_in[11]; a.w2 = (const u16*)d_in[12]; a.out = (float*)d_out;
  a.ws = (char*)d_ws;
  (void)hipFuncSetAttribute((const void*)s5_block, hipFuncAttributeMaxDynamicSharedMemorySize, FUSED_LDS);
  s5_block<<<256, 768, FUSED_LDS, stream>>>(a);
}
```
